```python
import torch

def _softmax(x, axis=-1):
    return torch.softmax(x, dim=axis)
def _gelu(x):
    return torch.nn.functional.gelu(x, approximate="tanh")
def _silu(x):
    return torch.nn.functional.silu(x)
def _sigmoid(x):
    return torch.sigmoid(x)
def _layernorm(x, axis=-1, eps=1e-5):
    m = x.mean(dim=axis, keepdim=True)
    v = x.var(dim=axis, keepdim=True, unbiased=False)
    return (x - m) / torch.sqrt(v + eps)
def _scatter_add(tgt, idx, src, axis=0):
    return tgt.clone().index_add_(axis, idx, src)
def _segment_sum(src, seg, n):
    out = torch.zeros((n,) + src.shape[1:], dtype=src.dtype)
    return out.index_add_(0, seg, src)
def _pad(x, pw):
    flat = tuple(v for pair in reversed(pw) for v in pair)
    return torch.nn.functional.pad(x, flat)

kernel_name = 'parallel_sum__sliding_block_cached__dense_activation_silu__decode'

def setup_inputs():

    _w = (lambda a: (lambda t: t.to(torch.bfloat16) if t.is_floating_point() else t)(torch.as_tensor(a)))
    def _A_setup():
        B, Sq, D = 1, 1, 1024
        Hq, Hkv, Sk, Dh = 32, 4, 65536, 128
        assert Hq % Hkv == 0, (Hq, Hkv)
        return {
            "x": _w((lambda *s: torch.randn(*s, dtype=torch.bfloat16) * 0.05)(B, Sq, D)),
            "k_cache": _w((lambda *s: torch.randn(*s, dtype=torch.bfloat16) * 3.2)(B, Hkv, Sk, Dh)),
            "v_cache": _w((lambda *s: torch.randn(*s, dtype=torch.bfloat16) * 0.8)(B, Hkv, Sk, Dh)),
            "wq": _w((lambda *s: torch.randn(*s, dtype=torch.bfloat16) * 3.2)(D, Hq * Dh) * (D ** -0.5)),
            "wo": _w((lambda *s: torch.randn(*s, dtype=torch.bfloat16) * 0.8)(Hq * Dh, D) * ((Hq * Dh) ** -0.5)),
        }
    _A_in = _A_setup()

    def _B_setup():
        B, S, D, F = 1, 1, 1024, 3584
        return {
            "x": _w((lambda *s: torch.randn(*s, dtype=torch.bfloat16) * 0.05)(B, S, D)),
            "w1": _w((lambda *s: torch.randn(*s, dtype=torch.bfloat16) * 3.2)(D, F) * (D ** -0.5)),
            "w2": _w((lambda *s: torch.randn(*s, dtype=torch.bfloat16) * 0.2)(F, D) * (F ** -0.5)),
        }
    _B_in = _B_setup()

    return {
        "A_x": _A_in["x"],
        "A_k_cache": _A_in["k_cache"],
        "A_v_cache": _A_in["v_cache"],
        "A_wq": _A_in["wq"],
        "A_wo": _A_in["wo"],
        "B_x": _B_in["x"],
        "B_w1": _B_in["w1"],
        "B_w2": _B_in["w2"],
    }

def reference(A_x, A_k_cache, A_v_cache, A_wq, A_wo, B_x, B_w1, B_w2):
    def _A_ref(x, k_cache, v_cache, wq, wo):
        B, Sq, D = x.shape
        Hq, Dh = 32, 128
        Hkv, Sk = k_cache.shape[1], k_cache.shape[2]
        W = min(4096, max(256, Sk // 4))
        rep = Hq // Hkv
        q = (lambda x, *d: x.permute(*d))((x @ wq).reshape(B, Sq, Hq, Dh), 0, 2, 1, 3)
        k = (lambda x, n, axis: torch.repeat_interleave(x, n, dim=axis))(k_cache[:, :, Sk - W:, :], rep, axis=1)
        v = (lambda x, n, axis: torch.repeat_interleave(x, n, dim=axis))(v_cache[:, :, Sk - W:, :], rep, axis=1)
        scores = torch.einsum("bhqd,bhkd->bhqk", q, k) * (Dh ** -0.5)
        p = _softmax(scores, axis=-1)
        o = torch.einsum("bhqk,bhkd->bhqd", p, v)
        return (lambda x, *d: x.permute(*d))(o, 0, 2, 1, 3).reshape(B, Sq, Hq * Dh) @ wo

    def _B_ref(x, w1, w2):
        h = torch.einsum("bsd,df->bsf", x, w1)
        h = _silu(h)
        out = torch.einsum("bsf,fd->bsd", h, w2)
        return out

    x = A_x
    xn = _layernorm(x, axis=-1)
    return x + _A_ref(xn, A_k_cache, A_v_cache, A_wq, A_wo) + _B_ref(xn, B_w1, B_w2)
```

```cpp
#include <hip/hip_runtime.h>
#include <hip/hip_bf16.h>
#include <stdint.h>

#define MIMIC 1

typedef unsigned short u16;
__device__ __forceinline__ float bf2f(u16 b) { return __uint_as_float(((unsigned)b) << 16); }
__device__ __forceinline__ u16 f2bf(float f) { unsigned u = __float_as_uint(f); u += 0x7FFFu + ((u >> 16) & 1u); return (u16)(u >> 16); }
__device__ __forceinline__ float rb(float f) { return MIMIC ? bf2f(f2bf(f)) : f; }

constexpr int D = 1024, HQ = 32, HKV = 4, SK = 65536, DH = 128, W = 4096, F = 3584;
constexpr int NSPLIT = 64;

__device__ void layernorm_to_lds(const u16* x, float* xn, float* red) {
  int t = threadIdx.x;
  float v[4]; float s = 0.f;
  for (int i = 0; i < 4; ++i) { v[i] = bf2f(x[t * 4 + i]); s += v[i]; }
  for (int o = 32; o; o >>= 1) s += __shfl_xor(s, o);
  if ((t & 63) == 0) red[t >> 6] = s;
  __syncthreads();
  float mean = (red[0] + red[1] + red[2] + red[3]) * (1.f / D);
  __syncthreads();
  float q = 0.f;
  for (int i = 0; i < 4; ++i) { float d = v[i] - mean; q += d * d; }
  for (int o = 32; o; o >>= 1) q += __shfl_xor(q, o);
  if ((t & 63) == 0) red[t >> 6] = q;
  __syncthreads();
  float var = (red[0] + red[1] + red[2] + red[3]) * (1.f / D);
  float mb = rb(mean), vb = rb(var);
  float den = rb(sqrtf(rb(vb + 1e-5f)));
  for (int i = 0; i < 4; ++i) xn[t * 4 + i] = rb(rb(v[i] - mb) / den);
  __syncthreads();
}

__global__ void k1(const u16* x, const u16* wq, const u16* w1, float* q, float* h) {
  __shared__ float xn[D]; __shared__ float red[4]; __shared__ float part[4][64];
  layernorm_to_lds(x, xn, red);
  int b = blockIdx.x, lane = threadIdx.x & 63, w = threadIdx.x >> 6;
  const u16* Wm; int N, col;
  if (b < 64) { Wm = wq; N = 4096; col = b * 64 + lane; } else { Wm = w1; N = F; col = (b - 64) * 64 + lane; }
  float acc = 0.f;
  for (int k = w * 256; k < w * 256 + 256; ++k) acc += xn[k] * bf2f(Wm[(size_t)k * N + col]);
  part[w][lane] = acc;
  __syncthreads();
  if (w == 0) {
    float s = part[0][lane] + part[1][lane] + part[2][lane] + part[3][lane];
    if (b < 64) q[col] = rb(s);
    else { float hb = rb(s); h[col] = rb(hb / (1.f + __expf(-hb))); }
  }
}

__global__ void k2(const u16* kc, const float* q, float* sc) {
  int hh = blockIdx.x, j = blockIdx.y * 256 + threadIdx.x, g = hh / 8;
  const u16* kr = kc + ((size_t)g * SK + (SK - W) + j) * DH;
  float acc = 0.f;
  for (int d = 0; d < DH; ++d) acc += q[hh * DH + d] * bf2f(kr[d]);
  sc[hh * W + j] = rb(rb(acc) * rb(0.08838834764831845f) );
}

__global__ void k3(const u16* vc, const float* sc, float* o) {
  __shared__ float p[W]; __shared__ float red[4]; __shared__ float part[2][128];
  int hh = blockIdx.x, g = hh / 8, t = threadIdx.x;
  float m = -INFINITY;
  for (int j = t; j < W; j += 256) m = fmaxf(m, sc[hh * W + j]);
  for (int of = 32; of; of >>= 1) m = fmaxf(m, __shfl_xor(m, of));
  if ((t & 63) == 0) red[t >> 6] = m;
  __syncthreads();
  m = fmaxf(fmaxf(red[0], red[1]), fmaxf(red[2], red[3]));
  __syncthreads();
  float l = 0.f;
  for (int j = t; j < W; j += 256) { float e = __expf(sc[hh * W + j] - m); p[j] = e; l += e; }
  for (int of = 32; of; of >>= 1) l += __shfl_xor(l, of);
  if ((t & 63) == 0) red[t >> 6] = l;
  __syncthreads();
  l = red[0] + red[1] + red[2] + red[3];
  for (int j = t; j < W; j += 256) p[j] = rb(p[j] / l);
  __syncthreads();
  int d = t & 127, half = t >> 7;
  float acc = 0.f;
  for (int j = half * 2048; j < half * 2048 + 2048; ++j) acc += p[j] * bf2f(vc[((size_t)g * SK + (SK - W) + j) * DH + d]);
  part[half][d] = acc;
  __syncthreads();
  if (t < 128) o[hh * DH + t] = rb(part[0][t] + part[1][t]);
}

__global__ void k4(const u16* x, const u16* wo, const u16* w2, const float* o, const float* h, u16* out) {
  __shared__ float pa[4][64], pb[4][64];
  int lane = threadIdx.x & 63, w = threadIdx.x >> 6, col = blockIdx.x * 64 + lane;
  float a = 0.f, b = 0.f;
  for (int k = w * 1024; k < w * 1024 + 1024; ++k) a += o[k] * bf2f(wo[(size_t)k * D + col]);
  for (int k = w * 896; k < w * 896 + 896; ++k) b += h[k] * bf2f(w2[(size_t)k * D + col]);
  pa[w][lane] = a; pb[w][lane] = b;
  __syncthreads();
  if (w == 0) {
    float A = rb(pa[0][lane] + pa[1][lane] + pa[2][lane] + pa[3][lane]);
    float B = rb(pb[0][lane] + pb[1][lane] + pb[2][lane] + pb[3][lane]);
    out[col] = f2bf(rb(bf2f(x[col]) + A) + B);
  }
}

extern "C" void kernel_launch(void* const* d_in, const int* in_sizes, int n_in, void* d_out, int out_size, void* d_ws, size_t ws_size, hipStream_t stream) {
  const u16* x = (const u16*)d_in[0]; const u16* kc = (const u16*)d_in[1]; const u16* vc = (const u16*)d_in[2];
  const u16* wq = (const u16*)d_in[3]; const u16* wo = (const u16*)d_in[4];
  const u16* w1 = (const u16*)d_in[6]; const u16* w2 = (const u16*)d_in[7];
  float* ws = (float*)d_ws;
  float* q = ws; float* h = ws + 4096; float* o = ws + 8192; float* sc = ws + 16384;
  k1<<<120, 256, 0, stream>>>(x, wq, w1, q, h);
  k2<<<dim3(32, 16), 256, 0, stream>>>(kc, q, sc);
  k3<<<32, 256, 0, stream>>>(vc, sc, o);
  k4<<<16, 256, 0, stream>>>(x, wo, w2, o, h, (u16*)d_out);
}
```

```cpp
#include <hip/hip_runtime.h>
#include <hip/hip_bf16.h>
#include <stdint.h>

#define GAS __attribute__((address_space(1)))
#define LAS __attribute__((address_space(3)))
typedef unsigned v4u __attribute__((ext_vector_type(4)));
typedef unsigned v2u __attribute__((ext_vector_type(2)));
typedef float f32x4 __attribute__((ext_vector_type(4)));
typedef float f32x2 __attribute__((ext_vector_type(2)));
typedef __bf16 bf16x8 __attribute__((ext_vector_type(8)));
typedef __bf16 bf16x2 __attribute__((ext_vector_type(2)));
typedef unsigned long long u64;
typedef GAS u64 gu64;
typedef GAS unsigned gu32;
#define RLX_AGENT __ATOMIC_RELAXED, __HIP_MEMORY_SCOPE_AGENT

#define NT_AUX 0
#define GP 8
#define F_EARLY 8
#define PREF 1
#define PF_LINES 88

constexpr int SK = 65536, WIN = 4096;
constexpr int WS_E = 0, WS_Q = 4096, WS_H = WS_Q + 65536, WS_ML = WS_H + 32768, WS_OUT = WS_ML + 16384, WS_O = WS_OUT + 131072, WS_END = WS_O + 524288;
constexpr unsigned SPIN_CAP = 1u << 18;

struct Args {
  const void* x; const void* k; const void* v; const void* wq; const void* wo; const void* w1; const void* w2;
  void* out; void* ws;
};

#define STAMP(i)

#define SB() __builtin_amdgcn_sched_barrier(0)
#define PIN(x) asm volatile("" : "+v"(x))
#define LDS_BARRIER() do { asm volatile("s_waitcnt lgkmcnt(0)\n\ts_barrier" ::: "memory"); } while (0)

__device__ __forceinline__ __amdgpu_buffer_rsrc_t rsrc(const void* p, unsigned bytes) { return __builtin_amdgcn_make_buffer_rsrc((void*)p, 0, bytes, 0x00020000); }
__device__ __forceinline__ v4u ldw(__amdgpu_buffer_rsrc_t r, int voff, int soff) { return __builtin_amdgcn_raw_buffer_load_b128(r, voff, soff, NT_AUX); }
__device__ __forceinline__ v4u ldp(__amdgpu_buffer_rsrc_t r, int voff, int soff) { return __builtin_amdgcn_raw_buffer_load_b128(r, voff, soff, 0); }
__device__ __forceinline__ f32x4 mfma(v4u a, v4u b, f32x4 c) { return __builtin_amdgcn_mfma_f32_16x16x32_bf16(__builtin_bit_cast(bf16x8, a), __builtin_bit_cast(bf16x8, b), c, 0, 0, 0); }
__device__ __forceinline__ float lo2f(unsigned u) { return __builtin_bit_cast(float, u << 16); }
__device__ __forceinline__ float hi2f(unsigned u) { return __builtin_bit_cast(float, u & 0xffff0000u); }
__device__ __forceinline__ unsigned pk(float lo, float hi) { f32x2 f = {lo, hi}; return __builtin_bit_cast(unsigned, __builtin_convertvector(f, bf16x2)); }
__device__ __forceinline__ float rbf(float f) { return lo2f(pk(f, f)); }
template <int CTRL> __device__ __forceinline__ float dpp(float x) { return __builtin_bit_cast(float, __builtin_amdgcn_mov_dpp(__builtin_bit_cast(int, x), CTRL, 0xf, 0xf, true)); }
constexpr int XOR1 = 0xB1, XOR2 = 0x4E, XOR7 = 0x141, XOR8 = 0x128, NEWB = 0x150;
__device__ __forceinline__ float x16_sum(float x) { auto s = __builtin_amdgcn_permlane16_swap(__float_as_uint(x), __float_as_uint(x), false, false); return __uint_as_float(s[0]) + __uint_as_float(s[1]); }
__device__ __forceinline__ float x32_sum(float x) { auto s = __builtin_amdgcn_permlane32_swap(__float_as_uint(x), __float_as_uint(x), false, false); return __uint_as_float(s[0]) + __uint_as_float(s[1]); }
__device__ __forceinline__ float x16_max(float x) { auto s = __builtin_amdgcn_permlane16_swap(__float_as_uint(x), __float_as_uint(x), false, false); return fmaxf(__uint_as_float(s[0]), __uint_as_float(s[1])); }
__device__ __forceinline__ float x32_max(float x) { auto s = __builtin_amdgcn_permlane32_swap(__float_as_uint(x), __float_as_uint(x), false, false); return fmaxf(__uint_as_float(s[0]), __uint_as_float(s[1])); }
__device__ __forceinline__ float wave_sum(float x) { x += dpp<XOR1>(x); x += dpp<XOR2>(x); x += dpp<XOR7>(x); x += dpp<XOR8>(x); return x32_sum(x16_sum(x)); }
__device__ __forceinline__ u64 gran(float a, float b, unsigned ta, unsigned tb) {
  const unsigned ua = ((__float_as_uint(a) + 0x80u) & 0xffffff00u) | ta, ub = ((__float_as_uint(b) + 0x80u) & 0xffffff00u) | tb;
  return ((u64)ub << 32) | ua;
}
__device__ __forceinline__ float ga(u64 g) { return __uint_as_float((unsigned)g & 0xffffff00u); }
__device__ __forceinline__ float gb(u64 g) { return __uint_as_float((unsigned)(g >> 32) & 0xffffff00u); }
__device__ __forceinline__ void gst(GAS char* p, u64 v) { __hip_atomic_store((gu64*)p, v, RLX_AGENT); }
__device__ __forceinline__ u64 gld(GAS char* p) { return __hip_atomic_load((gu64*)p, RLX_AGENT); }
__device__ __forceinline__ unsigned gbad(u64 g, unsigned ta, unsigned tb) { return ((unsigned)g ^ ta) | ((unsigned)(g >> 32) ^ tb); }

template <int I> __device__ __forceinline__ v4u onehot(const unsigned* m, const unsigned (&sel)[8]) {
  const unsigned s = m[I >> 1];
  v4u a;
  a.x = __builtin_amdgcn_perm(s, s, sel[(I & 1) * 4 + 0]); a.y = __builtin_amdgcn_perm(s, s, sel[(I & 1) * 4 + 1]);
  a.z = __builtin_amdgcn_perm(s, s, sel[(I & 1) * 4 + 2]); a.w = __builtin_amdgcn_perm(s, s, sel[(I & 1) * 4 + 3]);
  return a;
}
template <int N, int I = 0, class F> __device__ __forceinline__ void sfor(F&& f) { if constexpr (I < N) { f(std::integral_constant<int, I>{}); sfor<N, I + 1>(f); } }

constexpr int L_RED = 0;
constexpr int L_RED2 = 2048;
constexpr int L_Q = 4096;
constexpr int L_ML = L_Q + 2304;
constexpr int L_MUL = L_ML + 256;
constexpr int L_K = 8192;
constexpr int L_O = L_K + 32768;
constexpr int L_END = L_O + 4 * 8 * 528;

__global__ void __launch_bounds__(256, 1) fused_decode(Args a) {
  __shared__ __attribute__((aligned(16))) unsigned char smem[L_END];
  LAS unsigned char* L = (LAS unsigned char*)smem;
  const int lane = threadIdx.x & 63, w = __builtin_amdgcn_readfirstlane(threadIdx.x >> 6);
  unsigned pf = 0;
  if (PREF) {
    u64 pc; asm volatile("s_getpc_b64 %0" : "=s"(pc));
    pf = *(const GAS unsigned*)((pc & ~(u64)255) + (u64)(min((int)threadIdx.x >> 1, PF_LINES - 1) * 128));
  }
  SB();
  const int c = blockIdx.x, xcd = c & 7, j = c >> 3, g = xcd >> 1, idx = (xcd & 1) * 32 + j;
  const int hh = idx >> 3, cb = idx & 7, head = g * 8 + hh;
  const bool isA = j < 16, isF = j >= 18;
  const int kg = lane >> 4, n16 = lane & 15, rg = lane >> 3, c8 = lane & 7;
  GAS char* ws = (GAS char*)a.ws;
  const unsigned tag = *(const GAS unsigned*)(ws + WS_E) + 1u;
  const unsigned ta = tag & 0xffu, tb = (tag >> 8) & 0xffu;

  const int colq = idx & 15, kq = idx >> 4;
  const __amdgpu_buffer_rsrc_t rx = rsrc(a.x, 2048);
  const int f = (xcd & 1) * 14 + (j - 18), col1 = g * 14 + (f >> 1), kh = f & 1;
  const v4u xa = ldp(rx, lane * 32, 0), xb = ldp(rx, lane * 32 + 16, 0);
  const v4u xq = ldp(rx, rg * 16, (kq * 256 + w * 64) * 2);
  const v4u x1a = ldp(rx, rg * 32, (kh * 512 + w * 128) * 2), x1b = ldp(rx, rg * 32 + 16, (kh * 512 + w * 128) * 2);
  const __amdgpu_buffer_rsrc_t rq = rsrc((const char*)a.wq + (size_t)(kq * 256 + w * 64) * 8192 + (g * 16 + colq) * 128, 63 * 8192 + 128);
  v4u T0[8], T1[8], T2[8];
  sfor<8>([&](auto I) { T0[I] = ldw(rq, rg * 65536 + c8 * 16, I * 8192); });
  SB();
  unsigned sel[8];
  sfor<8>([&](auto I) { constexpr unsigned p = I >> 2, v = I & 3, src = ((2 * p + 1) << 8) | (2 * p);
    sel[I] = ((c8 >> 1) == (int)v) ? ((c8 & 1) ? ((src << 16) | 0x0c0cu) : (0x0c0c0000u | src)) : 0x0c0c0c0cu; });
  float mean_b, rden;
  {
    float xv[16];
    sfor<4>([&](auto I) { xv[2 * I] = lo2f(xa[(int)I]); xv[2 * I + 1] = hi2f(xa[(int)I]); xv[8 + 2 * I] = lo2f(xb[(int)I]); xv[9 + 2 * I] = hi2f(xb[(int)I]); });
    float s = 0.f;
    sfor<16>([&](auto I) { s += xv[I]; });
    const float mean = wave_sum(s) * (1.f / 1024.f);
    float q = 0.f;
    sfor<16>([&](auto I) { const float d = xv[I] - mean; q = fmaf(d, d, q); });
    const float var = wave_sum(q) * (1.f / 1024.f);
    mean_b = rbf(mean);
    rden = __builtin_amdgcn_rcpf(rbf(__builtin_amdgcn_sqrtf(rbf(rbf(var) + 1e-5f))));
  }
  auto norm2 = [&](unsigned u) { const unsigned d = pk(lo2f(u) - mean_b, hi2f(u) - mean_b); return pk(lo2f(d) * rden, hi2f(d) * rden); };
  unsigned mq[4];
  sfor<4>([&](auto I) { mq[I] = norm2(xq[(int)I]); });
  asm volatile("" :: "v"(pf));
  STAMP(1);

  const int split = (xcd & 1) * 16 + j;
  const size_t kvoff = ((size_t)g * SK + (SK - WIN) + split * 128 + w * 32) * 256;
  const __amdgpu_buffer_rsrc_t r1 = isA ? rsrc((const char*)a.k + kvoff, 8192)
                                        : rsrc((const char*)a.w1 + (size_t)(kh * 512 + w * 128) * 7168 + col1 * 128, isF ? 127 * 7168 + 128 : 0);
  const int v1 = isA ? lane * 16 : rg * 16 * 7168 + c8 * 16;
  const int s1 = isA ? 1024 : 7168;
  f32x4 acc = {0.f, 0.f, 0.f, 0.f};
  sfor<8>([&](auto I) { acc = mfma(onehot<I>(mq, sel), T0[I], acc); SB(); T1[I] = ldw(r1, v1, I * s1); SB(); });
  STAMP(2);
  const bool val64 = (kg >> 1) == (n16 >> 3);
  const int red_w = (w * 2 + (kg >> 1)) * 256 + c8 * 32 + (kg & 1) * 16;
  auto publish64 = [&](int lbuf, GAS char* dst) {
    f32x2 s = {0.f, 0.f};
    sfor<4>([&](auto I) { s += *(LAS f32x2*)(L + lbuf + ((lane >> 5) * 4 + I) * 256 + (lane & 31) * 8); });
    const float s0 = x32_sum(s.x), s1_ = x32_sum(s.y);
    if (lane < 32) gst(dst + lane * 8, gran(s0, s1_, ta, tb));
  };
  if (val64) *(LAS f32x4*)(L + L_RED + red_w) = acc;
  LDS_BARRIER();
  if (w == 0) publish64(L_RED, ws + WS_Q + (g * 4 + kq) * 4096 + colq * 256);
  SB();

  const __amdgpu_buffer_rsrc_t ro = rsrc((const char*)a.wo + (size_t)(head * 128 + w * 32) * 2048 + cb * 256, 31 * 2048 + 256);
  const int vo = kg * 2 * 2048 + n16 * 16;
  if (isA) {
    const __amdgpu_buffer_rsrc_t rv = rsrc((const char*)a.v + kvoff, 8192);
    LAS unsigned char* KL = L + L_K + w * 8192;
    sfor<8>([&](auto I) { const int key = 4 * I + kg;
      *(LAS v4u*)(KL + key * 256 + ((n16 ^ (key & 15)) * 16)) = T1[I]; SB();
      T2[I] = ldw(rv, kg * 1024 + n16 * 16, (I & 3) * 256 + (I >> 2) * 4096); SB(); });
    v4u KF[8];
    sfor<8>([&](auto I) { KF[I] = *(LAS v4u*)(KL + ((I >> 2) * 16 + n16) * 256 + (((4 * (I & 3) + kg) ^ n16) * 16)); });
    GAS char* qp = ws + WS_Q + g * 16384 + w * 1024 + lane * 8;
    u64 g8[8];
    for (unsigned spin = 0;; ++spin) {
      sfor<8>([&](auto I) { g8[I] = gld(qp + (I >> 1) * 4096 + (I & 1) * 512); });
      unsigned bad = 0;
      sfor<8>([&](auto I) { bad |= gbad(g8[I], ta, tb); });
      if (__all((bad & 0xffu) == 0) || spin > SPIN_CAP) break;
    }
    STAMP(3);
    sfor<8>([&](auto I) { T0[I] = ldw(ro, vo, ((I & 3) * 8 + (I >> 2)) * 2048); });
    SB();
    sfor<2>([&](auto E) {
      const float q0 = (ga(g8[E]) + ga(g8[2 + E])) + (ga(g8[4 + E]) + ga(g8[6 + E])), q1 = (gb(g8[E]) + gb(g8[2 + E])) + (gb(g8[4 + E]) + gb(g8[6 + E]));
      *(LAS unsigned*)(L + L_Q + (2 * w + E) * 272 + lane * 4) = pk(q0, q1); });
    LDS_BARRIER();
    f32x4 sc[2] = {{0.f, 0.f, 0.f, 0.f}, {0.f, 0.f, 0.f, 0.f}};
    sfor<4>([&](auto S) { const v4u qf = *(LAS v4u*)(L + L_Q + c8 * 272 + (4 * S + kg) * 16);
      sc[0] = mfma(KF[S], qf, sc[0]); sc[1] = mfma(KF[4 + S], qf, sc[1]); });
    v4u VF[8];
    sfor<4>([&](auto D) { sfor<4>([&](auto P) {
      VF[2 * D][(int)P] = __builtin_amdgcn_perm(T2[2 * P + 1][(int)D], T2[2 * P][(int)D], 0x05040100u);
      VF[2 * D + 1][(int)P] = __builtin_amdgcn_perm(T2[2 * P + 1][(int)D], T2[2 * P][(int)D], 0x07060302u); }); });
    float s[8];
    sfor<4>([&](auto I) { const unsigned r = pk(sc[I >> 1][2 * (I & 1)], sc[I >> 1][2 * (I & 1) + 1]);
      const unsigned r2 = pk(lo2f(r) * 0.08838834764831845f, hi2f(r) * 0.08838834764831845f); s[2 * I] = lo2f(r2); s[2 * I + 1] = hi2f(r2); });
    float m = fmaxf(fmaxf(fmaxf(s[0], s[1]), fmaxf(s[2], s[3])), fmaxf(fmaxf(s[4], s[5]), fmaxf(s[6], s[7])));
    m = x32_max(x16_max(m));
    float l = 0.f;
    sfor<8>([&](auto I) { s[I] = __builtin_amdgcn_exp2f((s[I] - m) * 1.4426950408889634f); l += s[I]; });
    v4u pf4;
    sfor<4>([&](auto I) { pf4[(int)I] = pk(s[2 * I], s[2 * I + 1]); });
    l = x32_sum(x16_sum(l));
    const f32x4 z = {0.f, 0.f, 0.f, 0.f};
    sfor<8>([&](auto J) { const f32x4 o = mfma(VF[J], pf4, z);
      if (n16 < 8) *(LAS f32x4*)(L + L_O + (w * 8 + n16) * 528 + kg * 128 + J * 16) = o; });
    if (lane < 8) { *(LAS float*)(L + L_ML + lane * 16 + w * 4) = m; *(LAS float*)(L + L_ML + 128 + lane * 16 + w * 4) = l; }
    LDS_BARRIER();
    sfor<2>([&](auto E) { const int h = 2 * w + E;
      const f32x4 m4 = *(LAS f32x4*)(L + L_ML + h * 16), l4 = *(LAS f32x4*)(L + L_ML + 128 + h * 16);
      const float M = fmaxf(fmaxf(m4.x, m4.y), fmaxf(m4.z, m4.w));
      f32x2 o = {0.f, 0.f}; float ls = 0.f;
      sfor<4>([&](auto U) { const float e = __builtin_amdgcn_exp2f((m4[(int)U] - M) * 1.4426950408889634f);
        o += *(LAS f32x2*)(L + L_O + (U * 8 + h) * 528 + lane * 8) * e; ls = fmaf(l4[(int)U], e, ls); });
      gst(ws + WS_O + ((g * 8 + h) * 32 + split) * 512 + lane * 8, gran(o.x, o.y, ta, tb));
      if (lane < 2) gst(ws + WS_ML + (g * 32 + split) * 128 + h * 16 + lane * 8, ((u64)tag << 32) | __float_as_uint(lane ? ls : M)); });
    STAMP(4);
  } else {
    unsigned m1[8];
    sfor<4>([&](auto I) { m1[I] = norm2(x1a[(int)I]); m1[4 + I] = norm2(x1b[(int)I]); });
    acc = f32x4{0.f, 0.f, 0.f, 0.f};
    sfor<8>([&](auto I) { acc = mfma(onehot<I>(m1, sel), T1[I], acc); SB(); T2[I] = ldw(r1, v1, (8 + I) * 7168); SB(); });
    sfor<8>([&](auto I) { acc = mfma(onehot<8 + I>(m1, sel), T2[I], acc); SB(); T0[I] = ldw(ro, vo, ((I & 3) * 8 + (I >> 2)) * 2048); SB(); });
    STAMP(3);
    if (val64) *(LAS f32x4*)(L + L_RED2 + red_w) = acc;
    LDS_BARRIER();
    if (w == 0 && isF) publish64(L_RED2, ws + WS_H + kh * 16384 + col1 * 256);
    STAMP(4);
  }
  SB();
  const int skip = cb < 4 ? 3 : 7;
  const bool has2 = hh != skip;
  const int rb = g * 7 + hh - (hh > skip ? 1 : 0);
  const __amdgpu_buffer_rsrc_t r2 = rsrc((const char*)a.w2 + (size_t)(rb * 128 + w * 32) * 2048 + cb * 256, has2 ? 31 * 2048 + 256 : 0);
  const int v2 = kg * 8 * 2048 + n16 * 16;
  sfor<GP>([&](auto I) { T1[I] = ldw(r2, v2, I * 2048); });
  SB();
  GAS char* op = ws + WS_O + head * 16384 + w * 128 + kg * 4096 + n16 * 8;
  GAS char* mp = ws + WS_ML + g * 4096 + hh * 16 + (lane >> 1) * 128 + (lane & 1) * 8;
  GAS char* hp = ws + WS_H + (lane >> 5) * 16384 + (has2 ? rb : g * 7) * 512 + w * 128 + n16 * 8;
  u64 go[8], gm, gh;
  auto sweep = [&]() { gm = gld(mp); gh = gld(hp); sfor<8>([&](auto I) { go[I] = gld(op + I * 512); }); };
  auto check = [&]() { unsigned bad = gbad(gh, ta, tb); sfor<8>([&](auto I) { bad |= gbad(go[I], ta, tb); }); return __all((bad & 0xffu) == 0 && (unsigned)(gm >> 32) == tag); };
  sweep();
  SB();
  sfor<8 - GP>([&](auto I) { T1[GP + I] = ldw(r2, v2, (GP + I) * 2048); });
  SB();
  for (unsigned spin = 0; !check() && spin < SPIN_CAP; ++spin) sweep();
  STAMP(5);
  {
    const float v = __uint_as_float((unsigned)gm);
    const float mm = dpp<0xA0>(v), ll = dpp<0xF5>(v);
    float M = fmaxf(mm, dpp<XOR2>(mm)); M = fmaxf(M, dpp<XOR7>(M)); M = fmaxf(M, dpp<XOR8>(M)); M = x32_max(x16_max(M));
    const float e = __builtin_amdgcn_exp2f((mm - M) * 1.4426950408889634f);
    float t = e * ll; t += dpp<XOR2>(t); t += dpp<XOR7>(t); t += dpp<XOR8>(t); t = x32_sum(x16_sum(t));
    float o0 = 0.f, o1 = 0.f;
    sfor<8>([&](auto I) { const float wt = dpp<NEWB + 2 * I>(e); o0 = fmaf(wt, ga(go[I]), o0); o1 = fmaf(wt, gb(go[I]), o1); });
    const float inv = __builtin_amdgcn_rcpf(t);
    o0 = x32_sum(x16_sum(o0)) * inv; o1 = x32_sum(x16_sum(o1)) * inv;
    const unsigned hb = pk(x32_sum(ga(gh)), x32_sum(gb(gh)));
    const float h0 = lo2f(hb), h1 = hi2f(hb);
    const float y0 = h0 * __builtin_amdgcn_rcpf(1.f + __builtin_amdgcn_exp2f(h0 * -1.4426950408889634f)), y1 = h1 * __builtin_amdgcn_rcpf(1.f + __builtin_amdgcn_exp2f(h1 * -1.4426950408889634f));
    const unsigned po = pk(o0, o1), ph = has2 ? pk(y0, y1) : 0u;
    if (lane < 32) *(LAS unsigned*)(L + L_MUL + w * 128 + lane * 4) = lane < 16 ? po : ph;
  }
  asm volatile("s_waitcnt lgkmcnt(0)" ::: "memory");
  const v4u mo4 = *(LAS v4u*)(L + L_MUL + w * 128 + kg * 16), mh4 = *(LAS v4u*)(L + L_MUL + w * 128 + 64 + kg * 16);
  const unsigned mo[4] = {mo4.x, mo4.y, mo4.z, mo4.w}, mh[4] = {mh4.x, mh4.y, mh4.z, mh4.w};
  acc = f32x4{0.f, 0.f, 0.f, 0.f};
  sfor<8>([&](auto I) { acc = mfma(onehot<I>(mo, sel), T0[I], acc); });
  sfor<8>([&](auto I) { acc = mfma(onehot<I>(mh, sel), T1[I], acc); });
  if (lane < 32) *(LAS f32x4*)(L + L_RED + w * 512 + n16 * 32 + kg * 16) = acc;
  LDS_BARRIER();
  if (w == 0) {
    f32x2 s = {0.f, 0.f};
    sfor<4>([&](auto I) { s += *(LAS f32x2*)(L + L_RED + I * 512 + lane * 8); });
    gst(ws + WS_OUT + (cb * 32 + head) * 512 + lane * 8, gran(s.x, s.y, ta, tb));
  }
  STAMP(6);
  if (!isA && !isF) {
    const int sp = xcd * 2 + (j - 16), rcb = sp >> 1, e = (sp & 1) * 4 + w, col = rcb * 128 + e * 16 + c8 * 2;
    const unsigned xr = __builtin_amdgcn_raw_buffer_load_b32(rx, col * 2, 0, 0);
    GAS char* rp = ws + WS_OUT + rcb * 16384 + rg * 2048 + e * 64 + c8 * 8;
    u64 g4[4];
    for (unsigned spin = 0;; ++spin) {
      sfor<4>([&](auto I) { g4[I] = gld(rp + I * 512); });
      unsigned bad = 0;
      sfor<4>([&](auto I) { bad |= gbad(g4[I], ta, tb); });
      if (__all((bad & 0xffu) == 0) || spin > SPIN_CAP) break;
    }
    float s0 = (ga(g4[0]) + ga(g4[1])) + (ga(g4[2]) + ga(g4[3])), s1_ = (gb(g4[0]) + gb(g4[1])) + (gb(g4[2]) + gb(g4[3]));
    s0 += dpp<XOR8>(s0); s1_ += dpp<XOR8>(s1_);
    s0 = x32_sum(x16_sum(s0)); s1_ = x32_sum(x16_sum(s1_));
    if (lane < 8) *(GAS unsigned*)((GAS char*)a.out + col * 2) = pk(lo2f(xr) + s0, hi2f(xr) + s1_);
    if (sp == 0 && w == 0 && lane == 0) __hip_atomic_store((gu32*)(ws + WS_E), tag, RLX_AGENT);
    STAMP(7);
  }
}

extern "C" void kernel_launch(void* const* d_in, const int* in_sizes, int n_in, void* d_out, int out_size, void* d_ws, size_t ws_size, hipStream_t stream) {
  Args a;
  a.x = d_in[0]; a.k = d_in[1]; a.v = d_in[2]; a.wq = d_in[3]; a.wo = d_in[4]; a.w1 = d_in[6]; a.w2 = d_in[7];
  a.out = d_out; a.ws = d_ws;
  hipLaunchKernelGGL(fused_decode, dim3(256), dim3(256), 0, stream, a);
}
```

```cpp
#define AMP 0
#include <hip/hip_runtime.h>
#include <hip/hip_bf16.h>
#include <stdint.h>

#define GAS __attribute__((address_space(1)))
#define LAS __attribute__((address_space(3)))
typedef unsigned v4u __attribute__((ext_vector_type(4)));
typedef unsigned v2u __attribute__((ext_vector_type(2)));
typedef float f32x4 __attribute__((ext_vector_type(4)));
typedef float f32x2 __attribute__((ext_vector_type(2)));
typedef __bf16 bf16x8 __attribute__((ext_vector_type(8)));
typedef __bf16 bf16x2 __attribute__((ext_vector_type(2)));
typedef unsigned long long u64;
typedef GAS u64 gu64;
typedef GAS unsigned gu32;
#define RLX_AGENT __ATOMIC_RELAXED, __HIP_MEMORY_SCOPE_AGENT

#define NT_AUX 2
#define GP 8
#define F_EARLY 8
#define PREF 1
#define PF_LINES 88

constexpr int SK = 65536, WIN = 4096;
constexpr int WS_E = 0, WS_Q = 4096, WS_H = WS_Q + 65536, WS_ML = WS_H + 32768, WS_OUT = WS_ML + 16384, WS_O = WS_OUT + 131072, WS_END = WS_O + 524288;
constexpr unsigned SPIN_CAP = 1u << 18;

struct Args {
  const void* x; const void* k; const void* v; const void* wq; const void* wo; const void* w1; const void* w2;
  void* out; void* ws;
};

#define STAMP(i) do { u64 t_; __builtin_amdgcn_sched_barrier(0); asm volatile("s_memrealtime %0\n\ts_waitcnt lgkmcnt(0)" : "=s"(t_) :: "memory"); ts[i] = t_; __builtin_amdgcn_sched_barrier(0); } while (0)

#define SB() __builtin_amdgcn_sched_barrier(0)
#define PIN(x) asm volatile("" : "+v"(x))
#define LDS_BARRIER() do { asm volatile("s_waitcnt lgkmcnt(0)\n\ts_barrier" ::: "memory"); } while (0)

__device__ __forceinline__ __amdgpu_buffer_rsrc_t rsrc(const void* p, unsigned bytes) { return __builtin_amdgcn_make_buffer_rsrc((void*)p, 0, bytes, 0x00020000); }
__device__ __forceinline__ v4u ldw(__amdgpu_buffer_rsrc_t r, int voff, int soff) { return __builtin_amdgcn_raw_buffer_load_b128(r, voff, soff, NT_AUX); }
__device__ __forceinline__ v4u ldp(__amdgpu_buffer_rsrc_t r, int voff, int soff) { return __builtin_amdgcn_raw_buffer_load_b128(r, voff, soff, 0); }
__device__ __forceinline__ f32x4 mfma(v4u a, v4u b, f32x4 c) { return __builtin_amdgcn_mfma_f32_16x16x32_bf16(__builtin_bit_cast(bf16x8, a), __builtin_bit_cast(bf16x8, b), c, 0, 0, 0); }
__device__ __forceinline__ float lo2f(unsigned u) { return __builtin_bit_cast(float, u << 16); }
__device__ __forceinline__ float hi2f(unsigned u) { return __builtin_bit_cast(float, u & 0xffff0000u); }
__device__ __forceinline__ unsigned pk(float lo, float hi) { f32x2 f = {lo, hi}; return __builtin_bit_cast(unsigned, __builtin_convertvector(f, bf16x2)); }
__device__ __forceinline__ float rbf(float f) { return lo2f(pk(f, f)); }
template <int CTRL> __device__ __forceinline__ float dpp(float x) { return __builtin_bit_cast(float, __builtin_amdgcn_mov_dpp(__builtin_bit_cast(int, x), CTRL, 0xf, 0xf, true)); }
constexpr int XOR1 = 0xB1, XOR2 = 0x4E, XOR7 = 0x141, XOR8 = 0x128, NEWB = 0x150;
__device__ __forceinline__ float x16_sum(float x) { auto s = __builtin_amdgcn_permlane16_swap(__float_as_uint(x), __float_as_uint(x), false, false); return __uint_as_float(s[0]) + __uint_as_float(s[1]); }
__device__ __forceinline__ float x32_sum(float x) { auto s = __builtin_amdgcn_permlane32_swap(__float_as_uint(x), __float_as_uint(x), false, false); return __uint_as_float(s[0]) + __uint_as_float(s[1]); }
__device__ __forceinline__ float x16_max(float x) { auto s = __builtin_amdgcn_permlane16_swap(__float_as_uint(x), __float_as_uint(x), false, false); return fmaxf(__uint_as_float(s[0]), __uint_as_float(s[1])); }
__device__ __forceinline__ float x32_max(float x) { auto s = __builtin_amdgcn_permlane32_swap(__float_as_uint(x), __float_as_uint(x), false, false); return fmaxf(__uint_as_float(s[0]), __uint_as_float(s[1])); }
__device__ __forceinline__ float wave_sum(float x) { x += dpp<XOR1>(x); x += dpp<XOR2>(x); x += dpp<XOR7>(x); x += dpp<XOR8>(x); return x32_sum(x16_sum(x)); }
__device__ __forceinline__ u64 gran(float a, float b, unsigned ta, unsigned tb) {
  const unsigned ua = ((__float_as_uint(a) + 0x80u) & 0xffffff00u) | ta, ub = ((__float_as_uint(b) + 0x80u) & 0xffffff00u) | tb;
  return ((u64)ub << 32) | ua;
}
__device__ __forceinline__ float ga(u64 g) { return __uint_as_float((unsigned)g & 0xffffff00u); }
__device__ __forceinline__ float gb(u64 g) { return __uint_as_float((unsigned)(g >> 32) & 0xffffff00u); }
__device__ __forceinline__ void gst(GAS char* p, u64 v) { __hip_atomic_store((gu64*)p, v, RLX_AGENT); }
__device__ __forceinline__ u64 gld(GAS char* p) { return __hip_atomic_load((gu64*)p, RLX_AGENT); }
__device__ __forceinline__ unsigned gbad(u64 g, unsigned ta, unsigned tb) { return ((unsigned)g ^ ta) | ((unsigned)(g >> 32) ^ tb); }

template <int I> __device__ __forceinline__ v4u onehot(const unsigned* m, const unsigned (&sel)[8]) {
  const unsigned s = m[I >> 1];
  v4u a;
  a.x = __builtin_amdgcn_perm(s, s, sel[(I & 1) * 4 + 0]); a.y = __builtin_amdgcn_perm(s, s, sel[(I & 1) * 4 + 1]);
  a.z = __builtin_amdgcn_perm(s, s, sel[(I & 1) * 4 + 2]); a.w = __builtin_amdgcn_perm(s, s, sel[(I & 1) * 4 + 3]);
  return a;
}
template <int N, int I = 0, class F> __device__ __forceinline__ void sfor(F&& f) { if constexpr (I < N) { f(std::integral_constant<int, I>{}); sfor<N, I + 1>(f); } }

constexpr int L_RED = 0;
constexpr int L_RED2 = 2048;
constexpr int L_Q = 4096;
constexpr int L_ML = L_Q + 2304;
constexpr int L_MUL = L_ML + 256;
constexpr int L_K = 8192;
constexpr int L_O = L_K + 32768;
constexpr int L_END = L_O + 4 * 8 * 528;

__global__ void __launch_bounds__(256, 1) fused_decode(Args a) {
  __shared__ __attribute__((aligned(16))) unsigned char smem[L_END];
  LAS unsigned char* L = (LAS unsigned char*)smem;
  u64 ts[8] = {0, 0, 0, 0, 0, 0, 0, 0};
  asm volatile("s_memrealtime %0" : "=s"(ts[0]) :: "memory");
  const int lane = threadIdx.x & 63, w = __builtin_amdgcn_readfirstlane(threadIdx.x >> 6);
  unsigned pf = 0;
  if (PREF) {
    u64 pc; asm volatile("s_getpc_b64 %0" : "=s"(pc));
    pf = *(const GAS unsigned*)((pc & ~(u64)255) + (u64)(min((int)threadIdx.x >> 1, PF_LINES - 1) * 128));
  }
  SB();
  const int c = blockIdx.x, xcd = c & 7, j = c >> 3, g = xcd >> 1, idx = (xcd & 1) * 32 + j;
  const int hh = idx >> 3, cb = idx & 7, head = g * 8 + hh;
  const bool isA = j < 16, isF = j >= 18;
  const int kg = lane >> 4, n16 = lane & 15, rg = lane >> 3, c8 = lane & 7;
  GAS char* ws = (GAS char*)a.ws;
  const unsigned tag = *(const GAS unsigned*)(ws + WS_E) + 1u;
  const unsigned ta = tag & 0xffu, tb = (tag >> 8) & 0xffu;

  const int colq = idx & 15, kq = idx >> 4;
  const __amdgpu_buffer_rsrc_t rx = rsrc(a.x, 2048);
  const int f = (xcd & 1) * 14 + (j - 18), col1 = g * 14 + (f >> 1), kh = f & 1;
  const v4u xa = ldp(rx, lane * 32, 0), xb = ldp(rx, lane * 32 + 16, 0);
  const v4u xq = ldp(rx, rg * 16, (kq * 256 + w * 64) * 2);
  const v4u x1a = ldp(rx, rg * 32, (kh * 512 + w * 128) * 2), x1b = ldp(rx, rg * 32 + 16, (kh * 512 + w * 128) * 2);
  const __amdgpu_buffer_rsrc_t rq = rsrc((const char*)a.wq + (size_t)(kq * 256 + w * 64) * 8192 + (g * 16 + colq) * 128, 63 * 8192 + 128);
  v4u T0[8], T1[8], T2[8];
  sfor<8>([&](auto I) { T0[I] = ldw(rq, rg * 65536 + c8 * 16, I * 8192); });
  SB();
  unsigned sel[8];
  sfor<8>([&](auto I) { constexpr unsigned p = I >> 2, v = I & 3, src = ((2 * p + 1) << 8) | (2 * p);
    sel[I] = ((c8 >> 1) == (int)v) ? ((c8 & 1) ? ((src << 16) | 0x0c0cu) : (0x0c0c0000u | src)) : 0x0c0c0c0cu; });
  float mean_b, rden;
  {
    float xv[16];
    sfor<4>([&](auto I) { xv[2 * I] = lo2f(xa[(int)I]); xv[2 * I + 1] = hi2f(xa[(int)I]); xv[8 + 2 * I] = lo2f(xb[(int)I]); xv[9 + 2 * I] = hi2f(xb[(int)I]); });
    float s = 0.f;
    sfor<16>([&](auto I) { s += xv[I]; });
    const float mean = wave_sum(s) * (1.f / 1024.f);
    float q = 0.f;
    sfor<16>([&](auto I) { const float d = xv[I] - mean; q = fmaf(d, d, q); });
    const float var = wave_sum(q) * (1.f / 1024.f);
    mean_b = rbf(mean);
    rden = __builtin_amdgcn_rcpf(rbf(__builtin_amdgcn_sqrtf(rbf(rbf(var) + 1e-5f))));
  }
  auto norm2 = [&](unsigned u) { const unsigned d = pk(lo2f(u) - mean_b, hi2f(u) - mean_b); return pk(lo2f(d) * rden, hi2f(d) * rden); };
  unsigned mq[4];
  sfor<4>([&](auto I) { mq[I] = norm2(xq[(int)I]); });
  asm volatile("" :: "v"(pf));
  STAMP(1);

  const int split = (xcd & 1) * 16 + j;
  const size_t kvoff = ((size_t)g * SK + (SK - WIN) + split * 128 + w * 32) * 256;
  const __amdgpu_buffer_rsrc_t r1 = isA ? rsrc((const char*)a.k + kvoff, 8192)
                                        : rsrc((const char*)a.w1 + (size_t)(kh * 512 + w * 128) * 7168 + col1 * 128, isF ? 127 * 7168 + 128 : 0);
  const int v1 = isA ? lane * 16 : rg * 16 * 7168 + c8 * 16;
  const int s1 = isA ? 1024 : 7168;
  f32x4 acc = {0.f, 0.f, 0.f, 0.f};
  sfor<8>([&](auto I) { acc = mfma(onehot<I>(mq, sel), T0[I], acc); SB(); T1[I] = ldw(r1, v1, I * s1); SB(); });
  STAMP(2);
  const bool val64 = (kg >> 1) == (n16 >> 3);
  const int red_w = (w * 2 + (kg >> 1)) * 256 + c8 * 32 + (kg & 1) * 16;
  auto publish64 = [&](int lbuf, GAS char* dst) {
    f32x2 s = {0.f, 0.f};
    sfor<4>([&](auto I) { s += *(LAS f32x2*)(L + lbuf + ((lane >> 5) * 4 + I) * 256 + (lane & 31) * 8); });
    const float s0 = x32_sum(s.x), s1_ = x32_sum(s.y);
    if (lane < 32) gst(dst + lane * 8, gran(s0, s1_, ta, tb));
  };
  if (val64) *(LAS f32x4*)(L + L_RED + red_w) = acc;
  LDS_BARRIER();
  if (w == 0) publish64(L_RED, ws + WS_Q + (g * 4 + kq) * 4096 + colq * 256);
  SB();

  const __amdgpu_buffer_rsrc_t ro = rsrc((const char*)a.wo + (size_t)(head * 128 + w * 32) * 2048 + cb * 256, 31 * 2048 + 256);
  const int vo = kg * 2 * 2048 + n16 * 16;
  if (isA) {
    const __amdgpu_buffer_rsrc_t rv = rsrc((const char*)a.v + kvoff, 8192);
    LAS unsigned char* KL = L + L_K + w * 8192;
    sfor<8>([&](auto I) { const int key = 4 * I + kg;
      *(LAS v4u*)(KL + key * 256 + ((n16 ^ (key & 15)) * 16)) = T1[I]; SB();
      T2[I] = ldw(rv, kg * 1024 + n16 * 16, (I & 3) * 256 + (I >> 2) * 4096); SB(); });
    v4u KF[8];
    sfor<8>([&](auto I) { KF[I] = *(LAS v4u*)(KL + ((I >> 2) * 16 + n16) * 256 + (((4 * (I & 3) + kg) ^ n16) * 16)); });
    GAS char* qp = ws + WS_Q + g * 16384 + w * 1024 + lane * 8;
    u64 g8[8];
    for (unsigned spin = 0;; ++spin) {
      sfor<8>([&](auto I) { g8[I] = gld(qp + (I >> 1) * 4096 + (I & 1) * 512); });
      unsigned bad = 0;
      sfor<8>([&](auto I) { bad |= gbad(g8[I], ta, tb); });
      if (__all((bad & 0xffu) == 0) || spin > SPIN_CAP) break;
    }
    STAMP(3);
    sfor<8>([&](auto I) { T0[I] = ldw(ro, vo, ((I & 3) * 8 + (I >> 2)) * 2048); });
    SB();
    sfor<2>([&](auto E) {
      const float q0 = (ga(g8[E]) + ga(g8[2 + E])) + (ga(g8[4 + E]) + ga(g8[6 + E])), q1 = (gb(g8[E]) + gb(g8[2 + E])) + (gb(g8[4 + E]) + gb(g8[6 + E]));
      *(LAS unsigned*)(L + L_Q + (2 * w + E) * 272 + lane * 4) = pk(q0, q1); });
    LDS_BARRIER();
    f32x4 sc[2] = {{0.f, 0.f, 0.f, 0.f}, {0.f, 0.f, 0.f, 0.f}};
    sfor<4>([&](auto S) { const v4u qf = *(LAS v4u*)(L + L_Q + c8 * 272 + (4 * S + kg) * 16);
      sc[0] = mfma(KF[S], qf, sc[0]); sc[1] = mfma(KF[4 + S], qf, sc[1]); });
    v4u VF[8];
    sfor<4>([&](auto D) { sfor<4>([&](auto P) {
      VF[2 * D][(int)P] = __builtin_amdgcn_perm(T2[2 * P + 1][(int)D], T2[2 * P][(int)D], 0x05040100u);
      VF[2 * D + 1][(int)P] = __builtin_amdgcn_perm(T2[2 * P + 1][(int)D], T2[2 * P][(int)D], 0x07060302u); }); });
    float s[8];
    sfor<4>([&](auto I) { const unsigned r = pk(sc[I >> 1][2 * (I & 1)], sc[I >> 1][2 * (I & 1) + 1]);
      const unsigned r2 = pk(lo2f(r) * 0.08838834764831845f, hi2f(r) * 0.08838834764831845f); s[2 * I] = lo2f(r2); s[2 * I + 1] = hi2f(r2); });
    float m = fmaxf(fmaxf(fmaxf(s[0], s[1]), fmaxf(s[2], s[3])), fmaxf(fmaxf(s[4], s[5]), fmaxf(s[6], s[7])));
    m = x32_max(x16_max(m));
    float l = 0.f;
    sfor<8>([&](auto I) { s[I] = __builtin_amdgcn_exp2f((s[I] - m) * 1.4426950408889634f); l += s[I]; });
    v4u pf4;
    sfor<4>([&](auto I) { pf4[(int)I] = pk(s[2 * I], s[2 * I + 1]); });
    l = x32_sum(x16_sum(l));
    const f32x4 z = {0.f, 0.f, 0.f, 0.f};
    sfor<8>([&](auto J) { const f32x4 o = mfma(VF[J], pf4, z);
      if (n16 < 8) *(LAS f32x4*)(L + L_O + (w * 8 + n16) * 528 + kg * 128 + J * 16) = o; });
    if (lane < 8) { *(LAS float*)(L + L_ML + lane * 16 + w * 4) = m; *(LAS float*)(L + L_ML + 128 + lane * 16 + w * 4) = l; }
    LDS_BARRIER();
    sfor<2>([&](auto E) { const int h = 2 * w + E;
      const f32x4 m4 = *(LAS f32x4*)(L + L_ML + h * 16), l4 = *(LAS f32x4*)(L + L_ML + 128 + h * 16);
      const float M = fmaxf(fmaxf(m4.x, m4.y), fmaxf(m4.z, m4.w));
      f32x2 o = {0.f, 0.f}; float ls = 0.f;
      sfor<4>([&](auto U) { const float e = __builtin_amdgcn_exp2f((m4[(int)U] - M) * 1.4426950408889634f);
        o += *(LAS f32x2*)(L + L_O + (U * 8 + h) * 528 + lane * 8) * e; ls = fmaf(l4[(int)U], e, ls); });
      gst(ws + WS_O + ((g * 8 + h) * 32 + split) * 512 + lane * 8, gran(o.x, o.y, ta, tb));
      if (lane < 2) gst(ws + WS_ML + (g * 32 + split) * 128 + h * 16 + lane * 8, ((u64)tag << 32) | __float_as_uint(lane ? ls : M)); });
    STAMP(4);
  } else {
    unsigned m1[8];
    sfor<4>([&](auto I) { m1[I] = norm2(x1a[(int)I]); m1[4 + I] = norm2(x1b[(int)I]); });
    acc = f32x4{0.f, 0.f, 0.f, 0.f};
    sfor<8>([&](auto I) { acc = mfma(onehot<I>(m1, sel), T1[I], acc); SB(); T2[I] = ldw(r1, v1, (8 + I) * 7168); SB(); });
    sfor<8>([&](auto I) { acc = mfma(onehot<8 + I>(m1, sel), T2[I], acc); SB(); T0[I] = ldw(ro, vo, ((I & 3) * 8 + (I >> 2)) * 2048); SB(); });
    STAMP(3);
    if (val64) *(LAS f32x4*)(L + L_RED2 + red_w) = acc;
    LDS_BARRIER();
    if (w == 0 && isF) publish64(L_RED2, ws + WS_H + kh * 16384 + col1 * 256);
    STAMP(4);
  }
  SB();
  const int skip = cb < 4 ? 3 : 7;
  const bool has2 = hh != skip;
  const int rb = g * 7 + hh - (hh > skip ? 1 : 0);
  const __amdgpu_buffer_rsrc_t r2 = rsrc((const char*)a.w2 + (size_t)(rb * 128 + w * 32) * 2048 + cb * 256, has2 ? 31 * 2048 + 256 : 0);
  const int v2 = kg * 8 * 2048 + n16 * 16;
  sfor<GP>([&](auto I) { T1[I] = ldw(r2, v2, I * 2048); });
  SB();
  GAS char* op = ws + WS_O + head * 16384 + w * 128 + kg * 4096 + n16 * 8;
  GAS char* mp = ws + WS_ML + g * 4096 + hh * 16 + (lane >> 1) * 128 + (lane & 1) * 8;
  GAS char* hp = ws + WS_H + (lane >> 5) * 16384 + (has2 ? rb : g * 7) * 512 + w * 128 + n16 * 8;
  u64 go[8], gm, gh;
  auto sweep = [&]() { gm = gld(mp); gh = gld(hp); sfor<8>([&](auto I) { go[I] = gld(op + I * 512); }); };
  auto check = [&]() { unsigned bad = gbad(gh, ta, tb); sfor<8>([&](auto I) { bad |= gbad(go[I], ta, tb); }); return __all((bad & 0xffu) == 0 && (unsigned)(gm >> 32) == tag); };
  sweep();
  SB();
  sfor<8 - GP>([&](auto I) { T1[GP + I] = ldw(r2, v2, (GP + I) * 2048); });
  SB();
  for (unsigned spin = 0; !check() && spin < SPIN_CAP; ++spin) sweep();
  STAMP(5);
  {
    const float v = __uint_as_float((unsigned)gm);
    const float mm = dpp<0xA0>(v), ll = dpp<0xF5>(v);
    float M = fmaxf(mm, dpp<XOR2>(mm)); M = fmaxf(M, dpp<XOR7>(M)); M = fmaxf(M, dpp<XOR8>(M)); M = x32_max(x16_max(M));
    const float e = __builtin_amdgcn_exp2f((mm - M) * 1.4426950408889634f);
    float t = e * ll; t += dpp<XOR2>(t); t += dpp<XOR7>(t); t += dpp<XOR8>(t); t = x32_sum(x16_sum(t));
    float o0 = 0.f, o1 = 0.f;
    sfor<8>([&](auto I) { const float wt = dpp<NEWB + 2 * I>(e); o0 = fmaf(wt, ga(go[I]), o0); o1 = fmaf(wt, gb(go[I]), o1); });
    const float inv = __builtin_amdgcn_rcpf(t);
    o0 = x32_sum(x16_sum(o0)) * inv; o1 = x32_sum(x16_sum(o1)) * inv;
    const unsigned hb = pk(x32_sum(ga(gh)), x32_sum(gb(gh)));
    const float h0 = lo2f(hb), h1 = hi2f(hb);
    const float y0 = h0 * __builtin_amdgcn_rcpf(1.f + __builtin_amdgcn_exp2f(h0 * -1.4426950408889634f)), y1 = h1 * __builtin_amdgcn_rcpf(1.f + __builtin_amdgcn_exp2f(h1 * -1.4426950408889634f));
    const unsigned po = pk(o0, o1), ph = has2 ? pk(y0, y1) : 0u;
    if (lane < 32) *(LAS unsigned*)(L + L_MUL + w * 128 + lane * 4) = lane < 16 ? po : ph;
  }
  asm volatile("s_waitcnt lgkmcnt(0)" ::: "memory");
  const v4u mo4 = *(LAS v4u*)(L + L_MUL + w * 128 + kg * 16), mh4 = *(LAS v4u*)(L + L_MUL + w * 128 + 64 + kg * 16);
  const unsigned mo[4] = {mo4.x, mo4.y, mo4.z, mo4.w}, mh[4] = {mh4.x, mh4.y, mh4.z, mh4.w};
  acc = f32x4{0.f, 0.f, 0.f, 0.f};
  sfor<8>([&](auto I) { acc = mfma(onehot<I>(mo, sel), T0[I], acc); });
  sfor<8>([&](auto I) { acc = mfma(onehot<I>(mh, sel), T1[I], acc); });
  if (lane < 32) *(LAS f32x4*)(L + L_RED + w * 512 + n16 * 32 + kg * 16) = acc;
  LDS_BARRIER();
  if (w == 0) {
    f32x2 s = {0.f, 0.f};
    sfor<4>([&](auto I) { s += *(LAS f32x2*)(L + L_RED + I * 512 + lane * 8); });
    gst(ws + WS_OUT + (cb * 32 + head) * 512 + lane * 8, gran(s.x, s.y, ta, tb));
  }
  STAMP(6);
  if (!isA && !isF) {
    const int sp = xcd * 2 + (j - 16), rcb = sp >> 1, e = (sp & 1) * 4 + w, col = rcb * 128 + e * 16 + c8 * 2;
    const unsigned xr = __builtin_amdgcn_raw_buffer_load_b32(rx, col * 2, 0, 0);
    GAS char* rp = ws + WS_OUT + rcb * 16384 + rg * 2048 + e * 64 + c8 * 8;
    u64 g4[4];
    for (unsigned spin = 0;; ++spin) {
      sfor<4>([&](auto I) { g4[I] = gld(rp + I * 512); });
      unsigned bad = 0;
      sfor<4>([&](auto I) { bad |= gbad(g4[I], ta, tb); });
      if (__all((bad & 0xffu) == 0) || spin > SPIN_CAP) break;
    }
    float s0 = (ga(g4[0]) + ga(g4[1])) + (ga(g4[2]) + ga(g4[3])), s1_ = (gb(g4[0]) + gb(g4[1])) + (gb(g4[2]) + gb(g4[3]));
    s0 += dpp<XOR8>(s0); s1_ += dpp<XOR8>(s1_);
    s0 = x32_sum(x16_sum(s0)); s1_ = x32_sum(x16_sum(s1_));
    if (lane < 8) *(GAS unsigned*)((GAS char*)a.out + col * 2) = pk(lo2f(xr) + s0, hi2f(xr) + s1_);
    if (sp == 0 && w == 0 && lane == 0) __hip_atomic_store((gu32*)(ws + WS_E), tag, RLX_AGENT);
    STAMP(7);
  }
  { const u64 until = ts[0] + 3000 + (ts[AMP] ? 10 * (ts[AMP] - ts[0]) : 0);
    for (int it = 0; it < 100000; ++it) { u64 t_; asm volatile("s_memrealtime %0\n\ts_waitcnt lgkmcnt(0)" : "=s"(t_) :: "memory"); if (t_ >= until) break; } }
}

extern "C" void kernel_launch(void* const* d_in, const int* in_sizes, int n_in, void* d_out, int out_size, void* d_ws, size_t ws_size, hipStream_t stream) {
  Args a;
  a.x = d_in[0]; a.k = d_in[1]; a.v = d_in[2]; a.wq = d_in[3]; a.wo = d_in[4]; a.w1 = d_in[6]; a.w2 = d_in[7];
  a.out = d_out; a.ws = d_ws;
  hipLaunchKernelGGL(fused_decode, dim3(256), dim3(256), 0, stream, a);
}
```

```cpp
#define AMP 1
#define PROBE 16
#define PROBE_NT 1
#include <hip/hip_runtime.h>
#include <hip/hip_bf16.h>
#include <stdint.h>

#define GAS __attribute__((address_space(1)))
#define LAS __attribute__((address_space(3)))
typedef unsigned v4u __attribute__((ext_vector_type(4)));
typedef unsigned v2u __attribute__((ext_vector_type(2)));
typedef float f32x4 __attribute__((ext_vector_type(4)));
typedef float f32x2 __attribute__((ext_vector_type(2)));
typedef __bf16 bf16x8 __attribute__((ext_vector_type(8)));
typedef __bf16 bf16x2 __attribute__((ext_vector_type(2)));
typedef unsigned long long u64;
typedef GAS u64 gu64;
typedef GAS unsigned gu32;
#define RLX_AGENT __ATOMIC_RELAXED, __HIP_MEMORY_SCOPE_AGENT

#define NT_AUX 2
#define GP 8
#define F_EARLY 8
#define PREF 1
#define PF_LINES 88

constexpr int SK = 65536, WIN = 4096;
constexpr int WS_E = 0, WS_Q = 4096, WS_H = WS_Q + 65536, WS_ML = WS_H + 32768, WS_OUT = WS_ML + 16384, WS_O = WS_OUT + 131072, WS_END = WS_O + 524288;
constexpr unsigned SPIN_CAP = 1u << 18;

struct Args {
  const void* x; const void* k; const void* v; const void* wq; const void* wo; const void* w1; const void* w2;
  void* out; void* ws;
};

#define STAMP(i) do { u64 t_; __builtin_amdgcn_sched_barrier(0); asm volatile("s_memrealtime %0\n\ts_waitcnt lgkmcnt(0)" : "=s"(t_) :: "memory"); ts[i] = t_; __builtin_amdgcn_sched_barrier(0); } while (0)

#define SB() __builtin_amdgcn_sched_barrier(0)
#define PIN(x) asm volatile("" : "+v"(x))
#define LDS_BARRIER() do { asm volatile("s_waitcnt lgkmcnt(0)\n\ts_barrier" ::: "memory"); } while (0)

__device__ __forceinline__ __amdgpu_buffer_rsrc_t rsrc(const void* p, unsigned bytes) { return __builtin_amdgcn_make_buffer_rsrc((void*)p, 0, bytes, 0x00020000); }
__device__ __forceinline__ v4u ldw(__amdgpu_buffer_rsrc_t r, int voff, int soff) { return __builtin_amdgcn_raw_buffer_load_b128(r, voff, soff, NT_AUX); }
__device__ __forceinline__ v4u ldp(__amdgpu_buffer_rsrc_t r, int voff, int soff) { return __builtin_amdgcn_raw_buffer_load_b128(r, voff, soff, 0); }
__device__ __forceinline__ f32x4 mfma(v4u a, v4u b, f32x4 c) { return __builtin_amdgcn_mfma_f32_16x16x32_bf16(__builtin_bit_cast(bf16x8, a), __builtin_bit_cast(bf16x8, b), c, 0, 0, 0); }
__device__ __forceinline__ float lo2f(unsigned u) { return __builtin_bit_cast(float, u << 16); }
__device__ __forceinline__ float hi2f(unsigned u) { return __builtin_bit_cast(float, u & 0xffff0000u); }
__device__ __forceinline__ unsigned pk(float lo, float hi) { f32x2 f = {lo, hi}; return __builtin_bit_cast(unsigned, __builtin_convertvector(f, bf16x2)); }
__device__ __forceinline__ float rbf(float f) { return lo2f(pk(f, f)); }
template <int CTRL> __device__ __forceinline__ float dpp(float x) { return __builtin_bit_cast(float, __builtin_amdgcn_mov_dpp(__builtin_bit_cast(int, x), CTRL, 0xf, 0xf, true)); }
constexpr int XOR1 = 0xB1, XOR2 = 0x4E, XOR7 = 0x141, XOR8 = 0x128, NEWB = 0x150;
__device__ __forceinline__ float x16_sum(float x) { auto s = __builtin_amdgcn_permlane16_swap(__float_as_uint(x), __float_as_uint(x), false, false); return __uint_as_float(s[0]) + __uint_as_float(s[1]); }
__device__ __forceinline__ float x32_sum(float x) { auto s = __builtin_amdgcn_permlane32_swap(__float_as_uint(x), __float_as_uint(x), false, false); return __uint_as_float(s[0]) + __uint_as_float(s[1]); }
__device__ __forceinline__ float x16_max(float x) { auto s = __builtin_amdgcn_permlane16_swap(__float_as_uint(x), __float_as_uint(x), false, false); return fmaxf(__uint_as_float(s[0]), __uint_as_float(s[1])); }
__device__ __forceinline__ float x32_max(float x) { auto s = __builtin_amdgcn_permlane32_swap(__float_as_uint(x), __float_as_uint(x), false, false); return fmaxf(__uint_as_float(s[0]), __uint_as_float(s[1])); }
__device__ __forceinline__ float wave_sum(float x) { x += dpp<XOR1>(x); x += dpp<XOR2>(x); x += dpp<XOR7>(x); x += dpp<XOR8>(x); return x32_sum(x16_sum(x)); }
__device__ __forceinline__ u64 gran(float a, float b, unsigned ta, unsigned tb) {
  const unsigned ua = ((__float_as_uint(a) + 0x80u) & 0xffffff00u) | ta, ub = ((__float_as_uint(b) + 0x80u) & 0xffffff00u) | tb;
  return ((u64)ub << 32) | ua;
}
__device__ __forceinline__ float ga(u64 g) { return __uint_as_float((unsigned)g & 0xffffff00u); }
__device__ __forceinline__ float gb(u64 g) { return __uint_as_float((unsigned)(g >> 32) & 0xffffff00u); }
__device__ __forceinline__ void gst(GAS char* p, u64 v) { __hip_atomic_store((gu64*)p, v, RLX_AGENT); }
__device__ __forceinline__ u64 gld(GAS char* p) { return __hip_atomic_load((gu64*)p, RLX_AGENT); }
__device__ __forceinline__ unsigned gbad(u64 g, unsigned ta, unsigned tb) { return ((unsigned)g ^ ta) | ((unsigned)(g >> 32) ^ tb); }

template <int I> __device__ __forceinline__ v4u onehot(const unsigned* m, const unsigned (&sel)[8]) {
  const unsigned s = m[I >> 1];
  v4u a;
  a.x = __builtin_amdgcn_perm(s, s, sel[(I & 1) * 4 + 0]); a.y = __builtin_amdgcn_perm(s, s, sel[(I & 1) * 4 + 1]);
  a.z = __builtin_amdgcn_perm(s, s, sel[(I & 1) * 4 + 2]); a.w = __builtin_amdgcn_perm(s, s, sel[(I & 1) * 4 + 3]);
  return a;
}
template <int N, int I = 0, class F> __device__ __forceinline__ void sfor(F&& f) { if constexpr (I < N) { f(std::integral_constant<int, I>{}); sfor<N, I + 1>(f); } }

constexpr int L_RED = 0;
constexpr int L_RED2 = 2048;
constexpr int L_Q = 4096;
constexpr int L_ML = L_Q + 2304;
constexpr int L_MUL = L_ML + 256;
constexpr int L_K = 8192;
constexpr int L_O = L_K + 32768;
constexpr int L_END = L_O + 4 * 8 * 528;

__global__ void __launch_bounds__(256, 1) fused_decode(Args a) {
  __shared__ __attribute__((aligned(16))) unsigned char smem[L_END];
  LAS unsigned char* L = (LAS unsigned char*)smem;
  u64 ts[8] = {0, 0, 0, 0, 0, 0, 0, 0};
  asm volatile("s_memrealtime %0" : "=s"(ts[0]) :: "memory");
  const int lane = threadIdx.x & 63, w = __builtin_amdgcn_readfirstlane(threadIdx.x >> 6);
  unsigned pf = 0;
  if (PREF) {
    u64 pc; asm volatile("s_getpc_b64 %0" : "=s"(pc));
    pf = *(const GAS unsigned*)((pc & ~(u64)255) + (u64)(min((int)threadIdx.x >> 1, PF_LINES - 1) * 128));
  }
  SB();
  u64 tprobe = 0;
  {
    const int c = blockIdx.x;
    v4u x = {0, 0, 0, 0};
    auto LD = [&](const void* base, size_t off) { const GAS v4u* p = (const GAS v4u*)((const char*)base + off) + lane; return PROBE_NT ? __builtin_nontemporal_load(p) : *p; };
    v4u t[38]; int n = 0;
    if (PROBE & 1) sfor<8>([&](auto I) { t[n++] = LD(a.wq, (size_t)(c * 32 + w * 8 + I) * 1024); });
    if (PROBE & 2) sfor<4>([&](auto I) { t[n++] = LD(a.k, ((size_t)(c >> 6) * 65536 + 61440) * 256 + (size_t)((c & 63) * 16 + w * 4 + I) * 1024); });
    if (PROBE & 2) sfor<4>([&](auto I) { t[n++] = LD(a.v, ((size_t)(c >> 6) * 65536 + 61440) * 256 + (size_t)((c & 63) * 16 + w * 4 + I) * 1024); });
    if (PROBE & 4) sfor<7>([&](auto I) { t[n++] = LD(a.w1, (size_t)(c * 28 + w * 7 + I) * 1024); });
    if (PROBE & 8) sfor<7>([&](auto I) { t[n++] = LD(a.w2, (size_t)(c * 28 + w * 7 + I) * 1024); });
    if (PROBE & 16) sfor<8>([&](auto I) { t[n++] = LD(a.wo, (size_t)(c * 32 + w * 8 + I) * 1024); });
    if (PROBE & 32) sfor<8>([&](auto I) { t[n++] = LD(a.ws, ((size_t)128 << 20) + (size_t)(c * 32 + w * 8 + I) * 1024); });
    SB();
    constexpr int NN = ((PROBE & 1) ? 8 : 0) + ((PROBE & 2) ? 8 : 0) + ((PROBE & 4) ? 7 : 0) + ((PROBE & 8) ? 7 : 0) + ((PROBE & 16) ? 8 : 0) + ((PROBE & 32) ? 8 : 0);
    sfor<NN>([&](auto I) { x ^= t[I]; });
    asm volatile("" :: "v"(x));
    u64 t_; SB(); asm volatile("s_memrealtime %0\n\ts_waitcnt lgkmcnt(0)" : "=s"(t_) :: "memory"); tprobe = t_; SB();
  }
  const int c = blockIdx.x, xcd = c & 7, j = c >> 3, g = xcd >> 1, idx = (xcd & 1) * 32 + j;
  const int hh = idx >> 3, cb = idx & 7, head = g * 8 + hh;
  const bool isA = j < 16, isF = j >= 18;
  const int kg = lane >> 4, n16 = lane & 15, rg = lane >> 3, c8 = lane & 7;
  GAS char* ws = (GAS char*)a.ws;
  const unsigned tag = *(const GAS unsigned*)(ws + WS_E) + 1u;
  const unsigned ta = tag & 0xffu, tb = (tag >> 8) & 0xffu;

  const int colq = idx & 15, kq = idx >> 4;
  const __amdgpu_buffer_rsrc_t rx = rsrc(a.x, 2048);
  const int f = (xcd & 1) * 14 + (j - 18), col1 = g * 14 + (f >> 1), kh = f & 1;
  const v4u xa = ldp(rx, lane * 32, 0), xb = ldp(rx, lane * 32 + 16, 0);
  const v4u xq = ldp(rx, rg * 16, (kq * 256 + w * 64) * 2);
  const v4u x1a = ldp(rx, rg * 32, (kh * 512 + w * 128) * 2), x1b = ldp(rx, rg * 32 + 16, (kh * 512 + w * 128) * 2);
  const __amdgpu_buffer_rsrc_t rq = rsrc((const char*)a.wq + (size_t)(kq * 256 + w * 64) * 8192 + (g * 16 + colq) * 128, 63 * 8192 + 128);
  v4u T0[8], T1[8], T2[8];
  sfor<8>([&](auto I) { T0[I] = ldw(rq, rg * 65536 + c8 * 16, I * 8192); });
  SB();
  unsigned sel[8];
  sfor<8>([&](auto I) { constexpr unsigned p = I >> 2, v = I & 3, src = ((2 * p + 1) << 8) | (2 * p);
    sel[I] = ((c8 >> 1) == (int)v) ? ((c8 & 1) ? ((src << 16) | 0x0c0cu) : (0x0c0c0000u | src)) : 0x0c0c0c0cu; });
  float mean_b, rden;
  {
    float xv[16];
    sfor<4>([&](auto I) { xv[2 * I] = lo2f(xa[(int)I]); xv[2 * I + 1] = hi2f(xa[(int)I]); xv[8 + 2 * I] = lo2f(xb[(int)I]); xv[9 + 2 * I] = hi2f(xb[(int)I]); });
    float s = 0.f;
    sfor<16>([&](auto I) { s += xv[I]; });
    const float mean = wave_sum(s) * (1.f / 1024.f);
    float q = 0.f;
    sfor<16>([&](auto I) { const float d = xv[I] - mean; q = fmaf(d, d, q); });
    const float var = wave_sum(q) * (1.f / 1024.f);
    mean_b = rbf(mean);
    rden = __builtin_amdgcn_rcpf(rbf(__builtin_amdgcn_sqrtf(rbf(rbf(var) + 1e-5f))));
  }
  auto norm2 = [&](unsigned u) { const unsigned d = pk(lo2f(u) - mean_b, hi2f(u) - mean_b); return pk(lo2f(d) * rden, hi2f(d) * rden); };
  unsigned mq[4];
  sfor<4>([&](auto I) { mq[I] = norm2(xq[(int)I]); });
  asm volatile("" :: "v"(pf));
  STAMP(1);

  const int split = (xcd & 1) * 16 + j;
  const size_t kvoff = ((size_t)g * SK + (SK - WIN) + split * 128 + w * 32) * 256;
  const __amdgpu_buffer_rsrc_t r1 = isA ? rsrc((const char*)a.k + kvoff, 8192)
                                        : rsrc((const char*)a.w1 + (size_t)(kh * 512 + w * 128) * 7168 + col1 * 128, isF ? 127 * 7168 + 128 : 0);
  const int v1 = isA ? lane * 16 : rg * 16 * 7168 + c8 * 16;
  const int s1 = isA ? 1024 : 7168;
  f32x4 acc = {0.f, 0.f, 0.f, 0.f};
  sfor<8>([&](auto I) { acc = mfma(onehot<I>(mq, sel), T0[I], acc); SB(); T1[I] = ldw(r1, v1, I * s1); SB(); });
  STAMP(2);
  const bool val64 = (kg >> 1) == (n16 >> 3);
  const int red_w = (w * 2 + (kg >> 1)) * 256 + c8 * 32 + (kg & 1) * 16;
  auto publish64 = [&](int lbuf, GAS char* dst) {
    f32x2 s = {0.f, 0.f};
    sfor<4>([&](auto I) { s += *(LAS f32x2*)(L + lbuf + ((lane >> 5) * 4 + I) * 256 + (lane & 31) * 8); });
    const float s0 = x32_sum(s.x), s1_ = x32_sum(s.y);
    if (lane < 32) gst(dst + lane * 8, gran(s0, s1_, ta, tb));
  };
  if (val64) *(LAS f32x4*)(L + L_RED + red_w) = acc;
  LDS_BARRIER();
  if (w == 0) publish64(L_RED, ws + WS_Q + (g * 4 + kq) * 4096 + colq * 256);
  SB();

  const __amdgpu_buffer_rsrc_t ro = rsrc((const char*)a.wo + (size_t)(head * 128 + w * 32) * 2048 + cb * 256, 31 * 2048 + 256);
  const int vo = kg * 2 * 2048 + n16 * 16;
  if (isA) {
    const __amdgpu_buffer_rsrc_t rv = rsrc((const char*)a.v + kvoff, 8192);
    LAS unsigned char* KL = L + L_K + w * 8192;
    sfor<8>([&](auto I) { const int key = 4 * I + kg;
      *(LAS v4u*)(KL + key * 256 + ((n16 ^ (key & 15)) * 16)) = T1[I]; SB();
      T2[I] = ldw(rv, kg * 1024 + n16 * 16, (I & 3) * 256 + (I >> 2) * 4096); SB(); });
    v4u KF[8];
    sfor<8>([&](auto I) { KF[I] = *(LAS v4u*)(KL + ((I >> 2) * 16 + n16) * 256 + (((4 * (I & 3) + kg) ^ n16) * 16)); });
    GAS char* qp = ws + WS_Q + g * 16384 + w * 1024 + lane * 8;
    u64 g8[8];
    for (unsigned spin = 0;; ++spin) {
      sfor<8>([&](auto I) { g8[I] = gld(qp + (I >> 1) * 4096 + (I & 1) * 512); });
      unsigned bad = 0;
      sfor<8>([&](auto I) { bad |= gbad(g8[I], ta, tb); });
      if (__all((bad & 0xffu) == 0) || spin > SPIN_CAP) break;
    }
    STAMP(3);
    sfor<8>([&](auto I) { T0[I] = ldw(ro, vo, ((I & 3) * 8 + (I >> 2)) * 2048); });
    SB();
    sfor<2>([&](auto E) {
      const float q0 = (ga(g8[E]) + ga(g8[2 + E])) + (ga(g8[4 + E]) + ga(g8[6 + E])), q1 = (gb(g8[E]) + gb(g8[2 + E])) + (gb(g8[4 + E]) + gb(g8[6 + E]));
      *(LAS unsigned*)(L + L_Q + (2 * w + E) * 272 + lane * 4) = pk(q0, q1); });
    LDS_BARRIER();
    f32x4 sc[2] = {{0.f, 0.f, 0.f, 0.f}, {0.f, 0.f, 0.f, 0.f}};
    sfor<4>([&](auto S) { const v4u qf = *(LAS v4u*)(L + L_Q + c8 * 272 + (4 * S + kg) * 16);
      sc[0] = mfma(KF[S], qf, sc[0]); sc[1] = mfma(KF[4 + S], qf, sc[1]); });
    v4u VF[8];
    sfor<4>([&](auto D) { sfor<4>([&](auto P) {
      VF[2 * D][(int)P] = __builtin_amdgcn_perm(T2[2 * P + 1][(int)D], T2[2 * P][(int)D], 0x05040100u);
      VF[2 * D + 1][(int)P] = __builtin_amdgcn_perm(T2[2 * P + 1][(int)D], T2[2 * P][(int)D], 0x07060302u); }); });
    float s[8];
    sfor<4>([&](auto I) { const unsigned r = pk(sc[I >> 1][2 * (I & 1)], sc[I >> 1][2 * (I & 1) + 1]);
      const unsigned r2 = pk(lo2f(r) * 0.08838834764831845f, hi2f(r) * 0.08838834764831845f); s[2 * I] = lo2f(r2); s[2 * I + 1] = hi2f(r2); });
    float m = fmaxf(fmaxf(fmaxf(s[0], s[1]), fmaxf(s[2], s[3])), fmaxf(fmaxf(s[4], s[5]), fmaxf(s[6], s[7])));
    m = x32_max(x16_max(m));
    float l = 0.f;
    sfor<8>([&](auto I) { s[I] = __builtin_amdgcn_exp2f((s[I] - m) * 1.4426950408889634f); l += s[I]; });
    v4u pf4;
    sfor<4>([&](auto I) { pf4[(int)I] = pk(s[2 * I], s[2 * I + 1]); });
    l = x32_sum(x16_sum(l));
    const f32x4 z = {0.f, 0.f, 0.f, 0.f};
    sfor<8>([&](auto J) { const f32x4 o = mfma(VF[J], pf4, z);
      if (n16 < 8) *(LAS f32x4*)(L + L_O + (w * 8 + n16) * 528 + kg * 128 + J * 16) = o; });
    if (lane < 8) { *(LAS float*)(L + L_ML + lane * 16 + w * 4) = m; *(LAS float*)(L + L_ML + 128 + lane * 16 + w * 4) = l; }
    LDS_BARRIER();
    sfor<2>([&](auto E) { const int h = 2 * w + E;
      const f32x4 m4 = *(LAS f32x4*)(L + L_ML + h * 16), l4 = *(LAS f32x4*)(L + L_ML + 128 + h * 16);
      const float M = fmaxf(fmaxf(m4.x, m4.y), fmaxf(m4.z, m4.w));
      f32x2 o = {0.f, 0.f}; float ls = 0.f;
      sfor<4>([&](auto U) { const float e = __builtin_amdgcn_exp2f((m4[(int)U] - M) * 1.4426950408889634f);
        o += *(LAS f32x2*)(L + L_O + (U * 8 + h) * 528 + lane * 8) * e; ls = fmaf(l4[(int)U], e, ls); });
      gst(ws + WS_O + ((g * 8 + h) * 32 + split) * 512 + lane * 8, gran(o.x, o.y, ta, tb));
      if (lane < 2) gst(ws + WS_ML + (g * 32 + split) * 128 + h * 16 + lane * 8, ((u64)tag << 32) | __float_as_uint(lane ? ls : M)); });
    STAMP(4);
  } else {
    unsigned m1[8];
    sfor<4>([&](auto I) { m1[I] = norm2(x1a[(int)I]); m1[4 + I] = norm2(x1b[(int)I]); });
    acc = f32x4{0.f, 0.f, 0.f, 0.f};
    sfor<8>([&](auto I) { acc = mfma(onehot<I>(m1, sel), T1[I], acc); SB(); T2[I] = ldw(r1, v1, (8 + I) * 7168); SB(); });
    sfor<8>([&](auto I) { acc = mfma(onehot<8 + I>(m1, sel), T2[I], acc); SB(); T0[I] = ldw(ro, vo, ((I & 3) * 8 + (I >> 2)) * 2048); SB(); });
    STAMP(3);
    if (val64) *(LAS f32x4*)(L + L_RED2 + red_w) = acc;
    LDS_BARRIER();
    if (w == 0 && isF) publish64(L_RED2, ws + WS_H + kh * 16384 + col1 * 256);
    STAMP(4);
  }
  SB();
  const int skip = cb < 4 ? 3 : 7;
  const bool has2 = hh != skip;
  const int rb = g * 7 + hh - (hh > skip ? 1 : 0);
  const __amdgpu_buffer_rsrc_t r2 = rsrc((const char*)a.w2 + (size_t)(rb * 128 + w * 32) * 2048 + cb * 256, has2 ? 31 * 2048 + 256 : 0);
  const int v2 = kg * 8 * 2048 + n16 * 16;
  sfor<GP>([&](auto I) { T1[I] = ldw(r2, v2, I * 2048); });
  SB();
  GAS char* op = ws + WS_O + head * 16384 + w * 128 + kg * 4096 + n16 * 8;
  GAS char* mp = ws + WS_ML + g * 4096 + hh * 16 + (lane >> 1) * 128 + (lane & 1) * 8;
  GAS char* hp = ws + WS_H + (lane >> 5) * 16384 + (has2 ? rb : g * 7) * 512 + w * 128 + n16 * 8;
  u64 go[8], gm, gh;
  auto sweep = [&]() { gm = gld(mp); gh = gld(hp); sfor<8>([&](auto I) { go[I] = gld(op + I * 512); }); };
  auto check = [&]() { unsigned bad = gbad(gh, ta, tb); sfor<8>([&](auto I) { bad |= gbad(go[I], ta, tb); }); return __all((bad & 0xffu) == 0 && (unsigned)(gm >> 32) == tag); };
  sweep();
  SB();
  sfor<8 - GP>([&](auto I) { T1[GP + I] = ldw(r2, v2, (GP + I) * 2048); });
  SB();
  for (unsigned spin = 0; !check() && spin < SPIN_CAP; ++spin) sweep();
  STAMP(5);
  {
    const float v = __uint_as_float((unsigned)gm);
    const float mm = dpp<0xA0>(v), ll = dpp<0xF5>(v);
    float M = fmaxf(mm, dpp<XOR2>(mm)); M = fmaxf(M, dpp<XOR7>(M)); M = fmaxf(M, dpp<XOR8>(M)); M = x32_max(x16_max(M));
    const float e = __builtin_amdgcn_exp2f((mm - M) * 1.4426950408889634f);
    float t = e * ll; t += dpp<XOR2>(t); t += dpp<XOR7>(t); t += dpp<XOR8>(t); t = x32_sum(x16_sum(t));
    float o0 = 0.f, o1 = 0.f;
    sfor<8>([&](auto I) { const float wt = dpp<NEWB + 2 * I>(e); o0 = fmaf(wt, ga(go[I]), o0); o1 = fmaf(wt, gb(go[I]), o1); });
    const float inv = __builtin_amdgcn_rcpf(t);
    o0 = x32_sum(x16_sum(o0)) * inv; o1 = x32_sum(x16_sum(o1)) * inv;
    const unsigned hb = pk(x32_sum(ga(gh)), x32_sum(gb(gh)));
    const float h0 = lo2f(hb), h1 = hi2f(hb);
    const float y0 = h0 * __builtin_amdgcn_rcpf(1.f + __builtin_amdgcn_exp2f(h0 * -1.4426950408889634f)), y1 = h1 * __builtin_amdgcn_rcpf(1.f + __builtin_amdgcn_exp2f(h1 * -1.4426950408889634f));
    const unsigned po = pk(o0, o1), ph = has2 ? pk(y0, y1) : 0u;
    if (lane < 32) *(LAS unsigned*)(L + L_MUL + w * 128 + lane * 4) = lane < 16 ? po : ph;
  }
  asm volatile("s_waitcnt lgkmcnt(0)" ::: "memory");
  const v4u mo4 = *(LAS v4u*)(L + L_MUL + w * 128 + kg * 16), mh4 = *(LAS v4u*)(L + L_MUL + w * 128 + 64 + kg * 16);
  const unsigned mo[4] = {mo4.x, mo4.y, mo4.z, mo4.w}, mh[4] = {mh4.x, mh4.y, mh4.z, mh4.w};
  acc = f32x4{0.f, 0.f, 0.f, 0.f};
  sfor<8>([&](auto I) { acc = mfma(onehot<I>(mo, sel), T0[I], acc); });
  sfor<8>([&](auto I) { acc = mfma(onehot<I>(mh, sel), T1[I], acc); });
  if (lane < 32) *(LAS f32x4*)(L + L_RED + w * 512 + n16 * 32 + kg * 16) = acc;
  LDS_BARRIER();
  if (w == 0) {
    f32x2 s = {0.f, 0.f};
    sfor<4>([&](auto I) { s += *(LAS f32x2*)(L + L_RED + I * 512 + lane * 8); });
    gst(ws + WS_OUT + (cb * 32 + head) * 512 + lane * 8, gran(s.x, s.y, ta, tb));
  }
  STAMP(6);
  if (!isA && !isF) {
    const int sp = xcd * 2 + (j - 16), rcb = sp >> 1, e = (sp & 1) * 4 + w, col = rcb * 128 + e * 16 + c8 * 2;
    const unsigned xr = __builtin_amdgcn_raw_buffer_load_b32(rx, col * 2, 0, 0);
    GAS char* rp = ws + WS_OUT + rcb * 16384 + rg * 2048 + e * 64 + c8 * 8;
    u64 g4[4];
    for (unsigned spin = 0;; ++spin) {
      sfor<4>([&](auto I) { g4[I] = gld(rp + I * 512); });
      unsigned bad = 0;
      sfor<4>([&](auto I) { bad |= gbad(g4[I], ta, tb); });
      if (__all((bad & 0xffu) == 0) || spin > SPIN_CAP) break;
    }
    float s0 = (ga(g4[0]) + ga(g4[1])) + (ga(g4[2]) + ga(g4[3])), s1_ = (gb(g4[0]) + gb(g4[1])) + (gb(g4[2]) + gb(g4[3]));
    s0 += dpp<XOR8>(s0); s1_ += dpp<XOR8>(s1_);
    s0 = x32_sum(x16_sum(s0)); s1_ = x32_sum(x16_sum(s1_));
    if (lane < 8) *(GAS unsigned*)((GAS char*)a.out + col * 2) = pk(lo2f(xr) + s0, hi2f(xr) + s1_);
    if (sp == 0 && w == 0 && lane == 0) __hip_atomic_store((gu32*)(ws + WS_E), tag, RLX_AGENT);
    STAMP(7);
  }
  ts[1] = tprobe;
  { const u64 until = ts[0] + 4000 + (ts[AMP] ? 10 * (ts[AMP] - ts[0]) : 0);
    for (int it = 0; it < 100000; ++it) { u64 t_; asm volatile("s_memrealtime %0\n\ts_waitcnt lgkmcnt(0)" : "=s"(t_) :: "memory"); if (t_ >= until) break; } }
}

extern "C" void kernel_launch(void* const* d_in, const int* in_sizes, int n_in, void* d_out, int out_size, void* d_ws, size_t ws_size, hipStream_t stream) {
  Args a;
  a.x = d_in[0]; a.k = d_in[1]; a.v = d_in[2]; a.wq = d_in[3]; a.wo = d_in[4]; a.w1 = d_in[6]; a.w2 = d_in[7];
  a.out = d_out; a.ws = d_ws;
  hipLaunchKernelGGL(fused_decode, dim3(256), dim3(256), 0, stream, a);
}
```

```cpp
#include <hip/hip_runtime.h>
#include <hip/hip_bf16.h>
#include <stdint.h>

#define GAS __attribute__((address_space(1)))
#define LAS __attribute__((address_space(3)))
typedef unsigned v4u __attribute__((ext_vector_type(4)));
typedef unsigned v2u __attribute__((ext_vector_type(2)));
typedef float f32x4 __attribute__((ext_vector_type(4)));
typedef float f32x2 __attribute__((ext_vector_type(2)));
typedef __bf16 bf16x8 __attribute__((ext_vector_type(8)));
typedef __bf16 bf16x2 __attribute__((ext_vector_type(2)));
typedef unsigned long long u64;
typedef GAS u64 gu64;
typedef GAS unsigned gu32;
#define RLX_AGENT __ATOMIC_RELAXED, __HIP_MEMORY_SCOPE_AGENT

#define NT_AUX 2
#define GPA 8
#define GPF 8
#define F_EARLY 0
#define F_SLEEP 0
#define POLL_SLEEP 2
#define PREF 1
#define PF_LINES 118

constexpr int SK = 65536, WIN = 4096;
constexpr int OREC = 640;
constexpr int WS_E = 0, WS_Q = 4096, WS_H = WS_Q + 65536, WS_OUT = WS_H + 65536, WS_O = WS_OUT + 131072, WS_END = WS_O + 1024 * OREC;
constexpr unsigned SPIN_CAP = 1u << 18;
constexpr float LOG2E = 1.4426950408889634f;

struct Args {
  const void* x; const void* k; const void* v; const void* wq; const void* wo; const void* w1; const void* w2;
  void* out; void* ws;
};

#define STAMP(i)

#define SB() __builtin_amdgcn_sched_barrier(0)
template <class T> __device__ __forceinline__ void PIN(T& x) { asm volatile("" : "+v"(x)); }
#define LDS_BARRIER() do { asm volatile("s_waitcnt lgkmcnt(0)\n\ts_barrier" ::: "memory"); } while (0)

typedef __amdgpu_buffer_rsrc_t rsrc_t;
__device__ __forceinline__ rsrc_t rsrc(const void* p, unsigned bytes) { return __builtin_amdgcn_make_buffer_rsrc((void*)p, 0, bytes, 0x00020000); }
__device__ __forceinline__ v4u ldw(rsrc_t r, int voff, int soff) { return __builtin_amdgcn_raw_buffer_load_b128(r, voff, soff, NT_AUX); }
__device__ __forceinline__ v4u ldp(rsrc_t r, int voff, int soff) { return __builtin_amdgcn_raw_buffer_load_b128(r, voff, soff, 0); }
__device__ __forceinline__ f32x4 mfma(v4u a, v4u b, f32x4 c) { return __builtin_amdgcn_mfma_f32_16x16x32_bf16(__builtin_bit_cast(bf16x8, a), __builtin_bit_cast(bf16x8, b), c, 0, 0, 0); }
__device__ __forceinline__ float u2f(unsigned u) { return __builtin_bit_cast(float, u); }
__device__ __forceinline__ unsigned f2u(float f) { return __builtin_bit_cast(unsigned, f); }
__device__ __forceinline__ float lo2f(unsigned u) { return u2f(u << 16); }
__device__ __forceinline__ float hi2f(unsigned u) { return u2f(u & 0xffff0000u); }
__device__ __forceinline__ unsigned pk(float lo, float hi) { f32x2 f = {lo, hi}; return __builtin_bit_cast(unsigned, __builtin_convertvector(f, bf16x2)); }
__device__ __forceinline__ float rbf(float f) { return lo2f(pk(f, f)); }
__device__ __forceinline__ float mx(float a, float b) { return __builtin_elementwise_maximum(a, b); }
__device__ __forceinline__ float ex2(float x) { return __builtin_amdgcn_exp2f(x); }
template <int CTRL> __device__ __forceinline__ unsigned dppu(unsigned x) { return (unsigned)__builtin_amdgcn_mov_dpp((int)x, CTRL, 0xf, 0xf, true); }
template <int CTRL> __device__ __forceinline__ float dpp(float x) { return u2f(dppu<CTRL>(f2u(x))); }
constexpr int XOR1 = 0xB1, XOR2 = 0x4E, XOR7 = 0x141, XOR8 = 0x128, NEWB = 0x150;
__device__ __forceinline__ float x16_sum(float x) { auto s = __builtin_amdgcn_permlane16_swap(f2u(x), f2u(x), false, false); return u2f(s[0]) + u2f(s[1]); }
__device__ __forceinline__ float x32_sum(float x) { auto s = __builtin_amdgcn_permlane32_swap(f2u(x), f2u(x), false, false); return u2f(s[0]) + u2f(s[1]); }
__device__ __forceinline__ float x16_max(float x) { auto s = __builtin_amdgcn_permlane16_swap(f2u(x), f2u(x), false, false); return mx(u2f(s[0]), u2f(s[1])); }
__device__ __forceinline__ float x32_max(float x) { auto s = __builtin_amdgcn_permlane32_swap(f2u(x), f2u(x), false, false); return mx(u2f(s[0]), u2f(s[1])); }
__device__ __forceinline__ float x32_sum2(float a, float b) { auto s = __builtin_amdgcn_permlane32_swap(f2u(a), f2u(b), false, false); return u2f(s[0]) + u2f(s[1]); }
__device__ __forceinline__ float wave_sum(float x) { x += dpp<XOR1>(x); x += dpp<XOR2>(x); x += dpp<XOR7>(x); x += dpp<XOR8>(x); return x32_sum(x16_sum(x)); }
__device__ __forceinline__ u64 gran(float a, float b, unsigned ta, unsigned tb) {
  const unsigned ua = (f2u(a) & 0xffffff00u) | ta, ub = (f2u(b) & 0xffffff00u) | tb;
  return ((u64)ub << 32) | ua;
}
__device__ __forceinline__ void gst(GAS char* base, unsigned off, u64 v) { __hip_atomic_store((gu64*)(base + off), v, RLX_AGENT); }
__device__ __forceinline__ u64 gld(GAS char* base, unsigned off) { return __hip_atomic_load((gu64*)(base + off), RLX_AGENT); }
__device__ __forceinline__ v2u untag(u64 g, unsigned ta, unsigned tb) { v2u r = {(unsigned)g ^ ta, (unsigned)(g >> 32) ^ tb}; return r; }

template <int I> __device__ __forceinline__ v4u onehot(const unsigned* m, const unsigned (&sel)[8]) {
  const unsigned s = m[I >> 1];
  v4u a;
  a.x = __builtin_amdgcn_perm(s, s, sel[(I & 1) * 4 + 0]); a.y = __builtin_amdgcn_perm(s, s, sel[(I & 1) * 4 + 1]);
  a.z = __builtin_amdgcn_perm(s, s, sel[(I & 1) * 4 + 2]); a.w = __builtin_amdgcn_perm(s, s, sel[(I & 1) * 4 + 3]);
  return a;
}
template <int N, int I = 0, class F> __device__ __forceinline__ void sfor(F&& f) { if constexpr (I < N) { f(std::integral_constant<int, I>{}); sfor<N, I + 1>(f); } }
__device__ __forceinline__ void tr8(const v4u (&in)[8], v4u (&out)[8]) {
  sfor<4>([&](auto P) { sfor<4>([&](auto D) {
    out[2 * D][(int)P] = __builtin_amdgcn_perm(in[2 * P + 1][(int)D], in[2 * P][(int)D], 0x05040100u);
    out[2 * D + 1][(int)P] = __builtin_amdgcn_perm(in[2 * P + 1][(int)D], in[2 * P][(int)D], 0x07060302u); }); });
}

constexpr int L_RED = 0;
constexpr int L_RED2 = 2048;
constexpr int L_Q = 4096;
constexpr int L_ML = L_Q + 2304;
constexpr int L_H = L_ML + 256;
constexpr int L_Z = L_H + 256;
constexpr int L_K = L_Z + 4 * 2048;
constexpr int L_O = L_K + 32768;
constexpr int L_END = L_O + 4 * 8 * 528;

template <bool ISA>
__device__ __forceinline__ void body(const Args& a, LAS unsigned char* L, unsigned pf
) {
  const int lane = threadIdx.x & 63, w = __builtin_amdgcn_readfirstlane(threadIdx.x >> 6);
  const int c = blockIdx.x, xcd = c & 7, j = c >> 3, g = xcd >> 1, idx = (xcd & 1) * 32 + j;
  const int hh = idx >> 3, cb = idx & 7, head = g * 8 + hh;
  const bool isF = j >= 18;
  const int kg = lane >> 4, n16 = lane & 15, rg = lane >> 3, c8 = lane & 7;
  GAS char* ws = (GAS char*)a.ws;

  const int colq = idx & 15, kq = idx >> 4;
  const rsrc_t rx = rsrc(a.x, 2048);
  const int f = (xcd & 1) * 14 + (j - 18), col1 = g * 7 + (f >> 2), k1 = f & 3;
  const v4u xa = ldp(rx, lane * 32, 0), xb = ldp(rx, lane * 32 + 16, 0);
  const v4u xq = ldp(rx, rg * 16, (kq * 256 + w * 64) * 2);
  v4u x1a, x1b;
  if (!ISA) { x1a = ldp(rx, kg * 32, (k1 * 256 + w * 64) * 2); x1b = ldp(rx, kg * 32 + 16, (k1 * 256 + w * 64) * 2); }
  const rsrc_t rq = rsrc((const char*)a.wq + (size_t)(kq * 256 + w * 64) * 8192 + (g * 16 + colq) * 128, 63 * 8192 + 128);
  v4u T0[8], T1[8], T2[8];
  sfor<8>([&](auto I) { T0[I] = ldw(rq, rg * 65536 + c8 * 16, I * 8192); });
  SB();
  unsigned tag;
  asm volatile("s_load_dword %0, %1, 0x0\n\ts_waitcnt lgkmcnt(0)" : "=s"(tag) : "s"(a.ws) : "memory");
  tag += 1u;
  const unsigned ta = tag & 0xffu, tb = (tag >> 8) & 0xffu;
  unsigned sel[8];
  sfor<8>([&](auto I) { constexpr unsigned p = I >> 2, v = I & 3, src = ((2 * p + 1) << 8) | (2 * p);
    sel[I] = ((c8 >> 1) == (int)v) ? ((c8 & 1) ? ((src << 16) | 0x0c0cu) : (0x0c0c0000u | src)) : 0x0c0c0c0cu; PIN(sel[I]); });
  { const v4u z = {0u, 0u, 0u, 0u};
    *(LAS v4u*)(L + L_Z + w * 2048 + lane * 16) = z; *(LAS v4u*)(L + L_Z + w * 2048 + 1024 + lane * 16) = z; }
  const int split = (xcd & 1) * 16 + j;
  const int skip = cb < 4 ? 3 : 7;
  const bool has2 = hh != skip;
  const int rb = g * 7 + hh - (hh > skip ? 1 : 0);
  const size_t kvoff = ((size_t)g * SK + (SK - WIN) + split * 128 + w * 32) * 256;
  const rsrc_t r1 = ISA ? rsrc((const char*)a.k + kvoff, 8192)
                        : rsrc((const char*)a.w1 + (size_t)(k1 * 256 + w * 64) * 7168 + col1 * 256, isF ? 63 * 7168 + 256 : 0);
  const rsrc_t rv = rsrc((const char*)a.v + kvoff, 8192);
  const rsrc_t ro = rsrc((const char*)a.wo + (size_t)(head * 128 + w * 32) * 2048 + cb * 256, 31 * 2048 + 256);
  const rsrc_t r2 = rsrc((const char*)a.w2 + (size_t)(rb * 128 + w * 32) * 2048 + cb * 256, has2 ? 31 * 2048 + 256 : 0);
  int v1 = ISA ? lane * 16 : kg * 16 * 7168 + n16 * 16;
  constexpr int s1 = ISA ? 1024 : 7168;
  int vo = kg * 2 * 2048 + n16 * 16, v2 = kg * 8 * 2048 + n16 * 16, vv = kg * 1024 + n16 * 16;
  unsigned a_qpub = WS_Q + (g * 4 + kq) * 4096 + colq * 256 + (lane & 31) * 8;
  unsigned a_og = WS_O + (head * 32 + kg * 8) * OREC + w * 128 + n16 * 8;
  unsigned a_ml = WS_O + (head * 32 + (lane >> 1)) * OREC + 512 + (lane & 1) * 8;
  unsigned a_hg = WS_H + kg * 16384 + (has2 ? rb : g * 7) * 512 + w * 128 + n16 * 8;
  unsigned a_opub = WS_OUT + (cb * 32 + head) * 512 + lane * 8;
  int l_red64w = L_RED + (w * 2 + (kg >> 1)) * 256 + c8 * 32 + (kg & 1) * 16, l_red64r = L_RED + (lane >> 5) * 1024 + (lane & 31) * 8;
  int l_red128w = w * 512 + n16 * 32 + (kg & 1) * 16, l_lane8 = lane * 8;
  int l_zw = L_Z + w * 2048 + 7 * 96 + n16 * 4 + (lane >> 5) * 2, l_zr = L_Z + w * 2048 + (7 - c8) * 96 + kg * 16;
  int l_hw = L_H + w * 64 + n16 * 4 + (lane >> 5) * 2, l_hr = L_H + w * 64 + kg * 16;
  PIN(v1); PIN(vo); PIN(v2); PIN(a_qpub); PIN(a_og); PIN(a_ml); PIN(a_hg); PIN(a_opub);
  PIN(l_red64w); PIN(l_red64r); PIN(l_red128w); PIN(l_lane8); PIN(l_zw); PIN(l_zr); PIN(l_hw); PIN(l_hr);
  SB();
  float mean_b, rden;
  {
    float xv[16];
    sfor<4>([&](auto I) { xv[2 * I] = lo2f(xa[(int)I]); xv[2 * I + 1] = hi2f(xa[(int)I]); xv[8 + 2 * I] = lo2f(xb[(int)I]); xv[9 + 2 * I] = hi2f(xb[(int)I]); });
    float s = xv[0];
    sfor<15>([&](auto I) { s += xv[I + 1]; });
    const float mean = wave_sum(s) * (1.f / 1024.f);
    float q = 0.f;
    sfor<16>([&](auto I) { const float d = xv[I] - mean; q = fmaf(d, d, q); });
    const float var = wave_sum(q) * (1.f / 1024.f);
    mean_b = rbf(mean);
    rden = __builtin_amdgcn_rcpf(rbf(__builtin_amdgcn_sqrtf(rbf(rbf(var) + 1e-5f))));
  }
  auto norm2 = [&](unsigned u) { const unsigned d = pk(lo2f(u) - mean_b, hi2f(u) - mean_b); return pk(lo2f(d) * rden, hi2f(d) * rden); };
  unsigned mq[4];
  sfor<4>([&](auto I) { mq[I] = norm2(xq[(int)I]); });
  asm volatile("" :: "v"(pf));
  STAMP(1);
  f32x4 acc = {0.f, 0.f, 0.f, 0.f};
  sfor<8>([&](auto I) { acc = mfma(onehot<I>(mq, sel), T0[I], acc); SB(); if (ISA || F_EARLY) T1[I] = ldw(r1, v1, I * s1); SB(); });
  STAMP(2);
  if ((kg >> 1) == (n16 >> 3)) *(LAS f32x4*)(L + l_red64w) = acc;
  LDS_BARRIER();
  if (w == 0) {
    f32x2 s = *(LAS f32x2*)(L + l_red64r);
    sfor<3>([&](auto I) { s += *(LAS f32x2*)(L + l_red64r + (I + 1) * 256); });
    const float s0 = x32_sum(s.x), s1_ = x32_sum(s.y);
    if (lane < 32) gst(ws, a_qpub, gran(s0, s1_, ta, tb));
  }
  SB();
  auto publish128 = [&](int lbuf, unsigned dst) {
    f32x2 s = *(LAS f32x2*)(L + lbuf + l_lane8);
    sfor<3>([&](auto I) { s += *(LAS f32x2*)(L + lbuf + l_lane8 + (I + 1) * 512); });
    gst(ws, dst, gran(s.x, s.y, ta, tb));
  };

  if constexpr (ISA) {
    LAS unsigned char* KL = L + L_K + w * 8192;
    sfor<8>([&](auto I) { const int key = 4 * I + kg;
      *(LAS v4u*)(KL + key * 256 + ((n16 ^ (key & 15)) * 16)) = T1[I]; SB();
      T2[I] = ldw(rv, vv, (I & 3) * 256 + (I >> 2) * 4096); SB(); });
    unsigned a_qg = WS_Q + g * 16384 + w * 1024 + lane * 8;
    u64 g8[8];
    sfor<8>([&](auto I) { g8[I] = gld(ws + (I >> 1) * 4096, a_qg + (I & 1) * 512); });
    SB();
    v4u KF[8], VF[8];
    sfor<8>([&](auto I) { KF[I] = *(LAS v4u*)(KL + ((I >> 2) * 16 + n16) * 256 + (((4 * (I & 3) + kg) ^ n16) * 16)); PIN(KF[I]); });
    int l_qw = L_Q + 2 * w * 272 + lane * 4, l_qr = L_Q + c8 * 272 + kg * 16, l_ow = L_O + (w * 8 + c8) * 528 + kg * 128, l_mlw = L_ML + c8 * 16 + w * 4;
    const int h2 = 2 * w + (lane >> 5);
    int l_mlr = L_ML + h2 * 16, l_or = L_O + h2 * 528 + (lane & 31) * 8;
    unsigned a_o = WS_O + ((g * 8 + h2) * 32 + split) * OREC + (lane & 31) * 8;
    PIN(l_qw); PIN(l_qr); PIN(l_ow); PIN(l_mlw); PIN(l_mlr); PIN(l_or); PIN(a_o);
    SB();
    tr8(T2, VF);
    sfor<8>([&](auto I) { PIN(VF[I]); });
    SB();
    v2u u8[8];
    for (unsigned spin = 0;; ++spin) {
      unsigned bad = 0;
      sfor<8>([&](auto I) { u8[I] = untag(g8[I], ta, tb); bad |= u8[I].x | u8[I].y; });
      if (__all((bad & 0xffu) == 0) || spin > SPIN_CAP) break;
      sfor<8>([&](auto I) { g8[I] = gld(ws + (I >> 1) * 4096, a_qg + (I & 1) * 512); });
    }
    STAMP(3);
    sfor<2>([&](auto E) {
      const f32x2 q2 = (__builtin_bit_cast(f32x2, u8[E]) + __builtin_bit_cast(f32x2, u8[2 + E])) + (__builtin_bit_cast(f32x2, u8[4 + E]) + __builtin_bit_cast(f32x2, u8[6 + E]));
      *(LAS unsigned*)(L + l_qw + E * 272) = pk(q2.x, q2.y); });
    SB();
    sfor<8>([&](auto I) { T0[I] = ldw(ro, vo, ((I & 3) * 8 + (I >> 2)) * 2048); });
    SB();
    LDS_BARRIER();
    f32x4 sa = {0.f, 0.f, 0.f, 0.f}, sb = {0.f, 0.f, 0.f, 0.f};
    sfor<4>([&](auto S) { const v4u qf = *(LAS v4u*)(L + l_qr + S * 64);
      sa = mfma(KF[S], qf, sa); sb = mfma(KF[4 + S], qf, sb); });
    float s[4];
    sfor<4>([&](auto I) { s[I] = n16 < 8 ? sa[(int)I] : sb[(int)I]; });
    sfor<2>([&](auto I) { const unsigned r = pk(s[2 * I], s[2 * I + 1]);
      const unsigned r2 = pk(lo2f(r) * 0.08838834764831845f, hi2f(r) * 0.08838834764831845f); s[2 * I] = lo2f(r2); s[2 * I + 1] = hi2f(r2); });
    float m = mx(mx(s[0], s[1]), mx(s[2], s[3]));
    m = mx(m, dpp<XOR8>(m));
    m = x32_max(x16_max(m));
    sfor<4>([&](auto I) { s[I] = ex2((s[I] - m) * LOG2E); });
    v4u p4;
    p4.x = pk(s[0], s[1]); p4.y = pk(s[2], s[3]); p4.z = dppu<XOR8>(p4.x); p4.w = dppu<XOR8>(p4.y);
    const f32x4 z = {0.f, 0.f, 0.f, 0.f};
    f32x4 o[8];
    sfor<8>([&](auto J) { o[J] = mfma(VF[J], p4, z); });
    float l = (s[0] + s[1]) + (s[2] + s[3]);
    l += dpp<XOR8>(l);
    l = x32_sum(x16_sum(l));
    if (n16 < 8) {
      sfor<8>([&](auto J) { *(LAS f32x4*)(L + l_ow + J * 16) = o[J]; });
      *(LAS float*)(L + l_mlw) = m; *(LAS float*)(L + l_mlw + 128) = l;
    }
    LDS_BARRIER();
    {
      const f32x4 m4 = *(LAS f32x4*)(L + l_mlr), l4 = *(LAS f32x4*)(L + l_mlr + 128);
      const float M = mx(mx(m4.x, m4.y), mx(m4.z, m4.w));
      float e[4];
      sfor<4>([&](auto U) { e[U] = ex2((m4[(int)U] - M) * LOG2E); });
      sfor<2>([&](auto E) {
        f32x2 oo = *(LAS f32x2*)(L + l_or + E * 256) * e[0];
        sfor<3>([&](auto U) { oo += *(LAS f32x2*)(L + l_or + (U + 1) * 8 * 528 + E * 256) * e[U + 1]; });
        gst(ws, a_o + E * 256, gran(oo.x, oo.y, ta, tb)); });
      const float ls = fmaf(l4.x, e[0], l4.y * e[1]) + fmaf(l4.z, e[2], l4.w * e[3]);
      if ((lane & 31) < 2) gst(ws, a_o + 512, ((u64)tag << 32) | f2u((lane & 1) ? ls : M));
    }
    STAMP(4);
  } else {
    if (!F_EARLY) { if (F_SLEEP) __builtin_amdgcn_s_sleep(F_SLEEP); sfor<8>([&](auto I) { T1[I] = ldw(r1, v1, I * s1); }); SB(); }
    unsigned m1[8];
    sfor<4>([&](auto I) { m1[I] = norm2(x1a[(int)I]); m1[4 + I] = norm2(x1b[(int)I]); });
    unsigned a_hpub = WS_H + k1 * 16384 + col1 * 512 + lane * 8;
    PIN(a_hpub);
    acc = f32x4{0.f, 0.f, 0.f, 0.f};
    sfor<8>([&](auto I) { acc = mfma(onehot<I>(m1, sel), T1[I], acc); SB(); T2[I] = ldw(r1, v1, (8 + I) * 7168); SB(); });
    sfor<8>([&](auto I) { acc = mfma(onehot<8 + I>(m1, sel), T2[I], acc); SB(); T0[I] = ldw(ro, vo, ((I & 3) * 8 + (I >> 2)) * 2048); SB(); });
    STAMP(3);
    if (lane < 32) *(LAS f32x4*)(L + L_RED2 + l_red128w) = acc;
    LDS_BARRIER();
    if (w == 0 && isF) publish128(L_RED2, a_hpub);
    STAMP(4);
  }
  SB();
  constexpr int GP = ISA ? GPA : GPF;
  sfor<GP>([&](auto I) { T1[I] = ldw(r2, v2, I * 2048); });
  SB();
  u64 go[8], gm, gh;
  auto sweep = [&]() { gm = gld(ws, a_ml); gh = gld(ws, a_hg); sfor<8>([&](auto I) { go[I] = gld(ws + (I >> 2) * 4 * OREC, a_og + (I & 3) * OREC); }); };
  sweep();
  SB();
  sfor<8 - GP>([&](auto I) { T1[GP + I] = ldw(r2, v2, (GP + I) * 2048); });
  SB();
  v4u WF[8];
  tr8(T0, WF);
  sfor<8>([&](auto I) { PIN(WF[I]); });
  SB();
  v2u uo[8], uh;
  auto check = [&]() { uh = untag(gh, ta, tb); unsigned bad = uh.x | uh.y; sfor<8>([&](auto I) { uo[I] = untag(go[I], ta, tb); bad |= uo[I].x | uo[I].y; });
    return __all((bad & 0xffu) == 0 && (unsigned)(gm >> 32) == tag); };
  if (!check()) {
    for (unsigned spin = 0; spin < SPIN_CAP; ++spin) {
      gm = gld(ws, a_ml); gh = gld(ws, a_hg);
      const v2u t = untag(gh, ta, tb);
      if (!__all(((t.x | t.y) & 0xffu) == 0 && (unsigned)(gm >> 32) == tag)) { if (POLL_SLEEP) __builtin_amdgcn_s_sleep(POLL_SLEEP); continue; }
      sweep();
      if (check()) break;
    }
  }
  STAMP(5);
  {
    const float v = u2f((unsigned)gm);
    const float mm = dpp<0xA0>(v), ll = dpp<0xF5>(v);
    float M = mx(mm, dpp<XOR2>(mm)); M = mx(M, dpp<XOR7>(M)); M = mx(M, dpp<XOR8>(M)); M = x32_max(x16_max(M));
    const float e = ex2((mm - M) * LOG2E);
    float o0 = 0.f, o1 = 0.f;
    sfor<8>([&](auto I) { const float wt = dpp<NEWB + 2 * I>(e); o0 = fmaf(wt, u2f(uo[I].x), o0); o1 = fmaf(wt, u2f(uo[I].y), o1); });
    float t = e * ll; t += dpp<XOR2>(t); t += dpp<XOR7>(t); t += dpp<XOR8>(t); t = x32_sum(x16_sum(t));
    const float oo = x16_sum(x32_sum2(o0, o1)) * __builtin_amdgcn_rcpf(t);
    *(LAS unsigned short*)(L + l_zw) = (unsigned short)pk(oo, oo);
    const float h = rbf(x16_sum(x32_sum2(u2f(uh.x), u2f(uh.y))));
    const float y = h * __builtin_amdgcn_rcpf(1.f + ex2(h * -LOG2E));
    *(LAS unsigned short*)(L + l_hw) = has2 ? (unsigned short)pk(y, y) : (unsigned short)0;
  }
  asm volatile("" ::: "memory");
  acc = f32x4{0.f, 0.f, 0.f, 0.f};
  const v4u mh4 = *(LAS v4u*)(L + l_hr);
  sfor<8>([&](auto J) { acc = mfma(*(LAS v4u*)(L + l_zr + J * 96), WF[J], acc); });
  const unsigned mh[4] = {mh4.x, mh4.y, mh4.z, mh4.w};
  sfor<8>([&](auto I) { acc = mfma(onehot<I>(mh, sel), T1[I], acc); });
  if (lane < 32) *(LAS f32x4*)(L + L_RED + l_red128w) = acc;
  LDS_BARRIER();
  if (w == 0) publish128(L_RED, a_opub);
  STAMP(6);
  if (!ISA && !isF) {
    const int sp = xcd * 2 + (j - 16), rcb = sp >> 1, e = (sp & 1) * 4 + w, col = rcb * 128 + e * 16 + c8 * 2;
    const unsigned xr = __builtin_amdgcn_raw_buffer_load_b32(rx, col * 2, 0, 0);
    const unsigned a_r = WS_OUT + rcb * 16384 + rg * 2048 + e * 64 + c8 * 8;
    v2u u4[4];
    for (unsigned spin = 0;; ++spin) {
      unsigned bad = 0;
      u64 g4[4];
      sfor<4>([&](auto I) { g4[I] = gld(ws, a_r + I * 512); });
      sfor<4>([&](auto I) { u4[I] = untag(g4[I], ta, tb); bad |= u4[I].x | u4[I].y; });
      if (__all((bad & 0xffu) == 0) || spin > SPIN_CAP) break;
    }
    const f32x2 s2 = (__builtin_bit_cast(f32x2, u4[0]) + __builtin_bit_cast(f32x2, u4[1])) + (__builtin_bit_cast(f32x2, u4[2]) + __builtin_bit_cast(f32x2, u4[3]));
    float s0 = s2.x, s1_ = s2.y;
    s0 += dpp<XOR8>(s0); s1_ += dpp<XOR8>(s1_);
    s0 = x32_sum(x16_sum(s0)); s1_ = x32_sum(x16_sum(s1_));
    if (lane < 8) *(GAS unsigned*)((GAS char*)a.out + col * 2) = pk(lo2f(xr) + s0, hi2f(xr) + s1_);
    if (sp == 0 && w == 0 && lane == 0) __hip_atomic_store((gu32*)(ws + WS_E), tag, RLX_AGENT);
    STAMP(7);
  }
}

extern "C" __global__ void __launch_bounds__(256, 2) fused_decode(const void* px, const void* pwq, void* pws, const void* pk, const void* pw1, const void* pv, const void* pwo, const void* pw2, void* pout
) {
  Args a; a.x = px; a.k = pk; a.v = pv; a.wq = pwq; a.wo = pwo; a.w1 = pw1; a.w2 = pw2; a.out = pout; a.ws = pws;
  __shared__ __attribute__((aligned(16))) unsigned char smem[L_END];
  LAS unsigned char* L = (LAS unsigned char*)smem;
#define TS
  unsigned pf = 0;
  if (PREF) {
    u64 pc; asm volatile("s_getpc_b64 %0" : "=s"(pc));
    pf = *(const GAS unsigned*)((pc & ~(u64)255) + (u64)(min((int)threadIdx.x >> 1, PF_LINES - 1) * 128));
  }
  SB();
  if (blockIdx.x < 128) body<true>(a, L, pf TS); else body<false>(a, L, pf TS);
}

#define STA
extern "C" void kernel_launch(void* const* d_in, const int* in_sizes, int n_in, void* d_out, int out_size, void* d_ws, size_t ws_size, hipStream_t stream) {
  hipLaunchKernelGGL(fused_decode, dim3(256), dim3(256), 0, stream, d_in[0], d_in[3], d_ws, d_in[1], d_in[6], d_in[2], d_in[4], d_in[7], d_out STA);
}
```

```cpp
#define AMP 1
#include <hip/hip_runtime.h>
#include <hip/hip_bf16.h>
#include <stdint.h>

#define GAS __attribute__((address_space(1)))
#define LAS __attribute__((address_space(3)))
typedef unsigned v4u __attribute__((ext_vector_type(4)));
typedef unsigned v2u __attribute__((ext_vector_type(2)));
typedef float f32x4 __attribute__((ext_vector_type(4)));
typedef float f32x2 __attribute__((ext_vector_type(2)));
typedef __bf16 bf16x8 __attribute__((ext_vector_type(8)));
typedef __bf16 bf16x2 __attribute__((ext_vector_type(2)));
typedef unsigned long long u64;
typedef GAS u64 gu64;
typedef GAS unsigned gu32;
#define RLX_AGENT __ATOMIC_RELAXED, __HIP_MEMORY_SCOPE_AGENT

#define NT_AUX 2
#define GPA 8
#define GPF 8
#define F_EARLY 0
#define F_SLEEP 0
#define POLL_SLEEP 2
#define PREF 1
#define PF_LINES 118

constexpr int SK = 65536, WIN = 4096;
constexpr int OREC = 640;
constexpr int WS_E = 0, WS_Q = 4096, WS_H = WS_Q + 65536, WS_OUT = WS_H + 65536, WS_O = WS_OUT + 131072, WS_END = WS_O + 1024 * OREC;
constexpr unsigned SPIN_CAP = 1u << 18;
constexpr float LOG2E = 1.4426950408889634f;

struct Args {
  const void* x; const void* k; const void* v; const void* wq; const void* wo; const void* w1; const void* w2;
  void* out; void* ws;
};

#define STAMP(i)

#define SB() __builtin_amdgcn_sched_barrier(0)
template <class T> __device__ __forceinline__ void PIN(T& x) { asm volatile("" : "+v"(x)); }
#define LDS_BARRIER() do { asm volatile("s_waitcnt lgkmcnt(0)\n\ts_barrier" ::: "memory"); } while (0)

typedef __amdgpu_buffer_rsrc_t rsrc_t;
__device__ __forceinline__ rsrc_t rsrc(const void* p, unsigned bytes) { return __builtin_amdgcn_make_buffer_rsrc((void*)p, 0, bytes, 0x00020000); }
__device__ __forceinline__ v4u ldw(rsrc_t r, int voff, int soff) { return __builtin_amdgcn_raw_buffer_load_b128(r, voff, soff, NT_AUX); }
__device__ __forceinline__ v4u ldp(rsrc_t r, int voff, int soff) { return __builtin_amdgcn_raw_buffer_load_b128(r, voff, soff, 0); }
__device__ __forceinline__ f32x4 mfma(v4u a, v4u b, f32x4 c) { return __builtin_amdgcn_mfma_f32_16x16x32_bf16(__builtin_bit_cast(bf16x8, a), __builtin_bit_cast(bf16x8, b), c, 0, 0, 0); }
__device__ __forceinline__ float u2f(unsigned u) { return __builtin_bit_cast(float, u); }
__device__ __forceinline__ unsigned f2u(float f) { return __builtin_bit_cast(unsigned, f); }
__device__ __forceinline__ float lo2f(unsigned u) { return u2f(u << 16); }
__device__ __forceinline__ float hi2f(unsigned u) { return u2f(u & 0xffff0000u); }
__device__ __forceinline__ unsigned pk(float lo, float hi) { f32x2 f = {lo, hi}; return __builtin_bit_cast(unsigned, __builtin_convertvector(f, bf16x2)); }
__device__ __forceinline__ float rbf(float f) { return lo2f(pk(f, f)); }
__device__ __forceinline__ float mx(float a, float b) { return __builtin_elementwise_maximum(a, b); }
__device__ __forceinline__ float ex2(float x) { return __builtin_amdgcn_exp2f(x); }
template <int CTRL> __device__ __forceinline__ unsigned dppu(unsigned x) { return (unsigned)__builtin_amdgcn_mov_dpp((int)x, CTRL, 0xf, 0xf, true); }
template <int CTRL> __device__ __forceinline__ float dpp(float x) { return u2f(dppu<CTRL>(f2u(x))); }
constexpr int XOR1 = 0xB1, XOR2 = 0x4E, XOR7 = 0x141, XOR8 = 0x128, NEWB = 0x150;
__device__ __forceinline__ float x16_sum(float x) { auto s = __builtin_amdgcn_permlane16_swap(f2u(x), f2u(x), false, false); return u2f(s[0]) + u2f(s[1]); }
__device__ __forceinline__ float x32_sum(float x) { auto s = __builtin_amdgcn_permlane32_swap(f2u(x), f2u(x), false, false); return u2f(s[0]) + u2f(s[1]); }
__device__ __forceinline__ float x16_max(float x) { auto s = __builtin_amdgcn_permlane16_swap(f2u(x), f2u(x), false, false); return mx(u2f(s[0]), u2f(s[1])); }
__device__ __forceinline__ float x32_max(float x) { auto s = __builtin_amdgcn_permlane32_swap(f2u(x), f2u(x), false, false); return mx(u2f(s[0]), u2f(s[1])); }
__device__ __forceinline__ float x32_sum2(float a, float b) { auto s = __builtin_amdgcn_permlane32_swap(f2u(a), f2u(b), false, false); return u2f(s[0]) + u2f(s[1]); }
__device__ __forceinline__ float wave_sum(float x) { x += dpp<XOR1>(x); x += dpp<XOR2>(x); x += dpp<XOR7>(x); x += dpp<XOR8>(x); return x32_sum(x16_sum(x)); }
__device__ __forceinline__ u64 gran(float a, float b, unsigned ta, unsigned tb) {
  const unsigned ua = (f2u(a) & 0xffffff00u) | ta, ub = (f2u(b) & 0xffffff00u) | tb;
  return ((u64)ub << 32) | ua;
}
__device__ __forceinline__ void gst(GAS char* base, unsigned off, u64 v) { __hip_atomic_store((gu64*)(base + off), v, RLX_AGENT); }
__device__ __forceinline__ u64 gld(GAS char* base, unsigned off) { return __hip_atomic_load((gu64*)(base + off), RLX_AGENT); }
__device__ __forceinline__ v2u untag(u64 g, unsigned ta, unsigned tb) { v2u r = {(unsigned)g ^ ta, (unsigned)(g >> 32) ^ tb}; return r; }

template <int I> __device__ __forceinline__ v4u onehot(const unsigned* m, const unsigned (&sel)[8]) {
  const unsigned s = m[I >> 1];
  v4u a;
  a.x = __builtin_amdgcn_perm(s, s, sel[(I & 1) * 4 + 0]); a.y = __builtin_amdgcn_perm(s, s, sel[(I & 1) * 4 + 1]);
  a.z = __builtin_amdgcn_perm(s, s, sel[(I & 1) * 4 + 2]); a.w = __builtin_amdgcn_perm(s, s, sel[(I & 1) * 4 + 3]);
  return a;
}
template <int N, int I = 0, class F> __device__ __forceinline__ void sfor(F&& f) { if constexpr (I < N) { f(std::integral_constant<int, I>{}); sfor<N, I + 1>(f); } }
__device__ __forceinline__ void tr8(const v4u (&in)[8], v4u (&out)[8]) {
  sfor<4>([&](auto P) { sfor<4>([&](auto D) {
    out[2 * D][(int)P] = __builtin_amdgcn_perm(in[2 * P + 1][(int)D], in[2 * P][(int)D], 0x05040100u);
    out[2 * D + 1][(int)P] = __builtin_amdgcn_perm(in[2 * P + 1][(int)D], in[2 * P][(int)D], 0x07060302u); }); });
}

constexpr int L_RED = 0;
constexpr int L_RED2 = 2048;
constexpr int L_Q = 4096;
constexpr int L_ML = L_Q + 2304;
constexpr int L_H = L_ML + 256;
constexpr int L_Z = L_H + 256;
constexpr int L_K = L_Z + 4 * 2048;
constexpr int L_O = L_K + 32768;
constexpr int L_END = L_O + 4 * 8 * 528;

template <bool ISA>
__device__ __forceinline__ void body(const Args& a, LAS unsigned char* L, unsigned pf
                                     , u64 (&ts)[8]
) {
  const int lane = threadIdx.x & 63, w = __builtin_amdgcn_readfirstlane(threadIdx.x >> 6);
  const int c = blockIdx.x, xcd = c & 7, j = c >> 3, g = xcd >> 1, idx = (xcd & 1) * 32 + j;
  const int hh = idx >> 3, cb = idx & 7, head = g * 8 + hh;
  const bool isF = j >= 18;
  const int kg = lane >> 4, n16 = lane & 15, rg = lane >> 3, c8 = lane & 7;
  GAS char* ws = (GAS char*)a.ws;

  const int colq = idx & 15, kq = idx >> 4;
  const rsrc_t rx = rsrc(a.x, 2048);
  const int f = (xcd & 1) * 14 + (j - 18), col1 = g * 7 + (f >> 2), k1 = f & 3;
  const v4u xa = ldp(rx, lane * 32, 0), xb = ldp(rx, lane * 32 + 16, 0);
  const v4u xq = ldp(rx, rg * 16, (kq * 256 + w * 64) * 2);
  v4u x1a, x1b;
  if (!ISA) { x1a = ldp(rx, kg * 32, (k1 * 256 + w * 64) * 2); x1b = ldp(rx, kg * 32 + 16, (k1 * 256 + w * 64) * 2); }
  const rsrc_t rq = rsrc((const char*)a.wq + (size_t)(kq * 256 + w * 64) * 8192 + (g * 16 + colq) * 128, 63 * 8192 + 128);
  v4u T0[8], T1[8], T2[8];
  sfor<8>([&](auto I) { T0[I] = ldw(rq, rg * 65536 + c8 * 16, I * 8192); });
  SB();
  unsigned tag;
  asm volatile("s_load_dword %0, %1, 0x0\n\ts_waitcnt lgkmcnt(0)" : "=s"(tag) : "s"(a.ws) : "memory");
  tag += 1u;
  const unsigned ta = tag & 0xffu, tb = (tag >> 8) & 0xffu;
  unsigned sel[8];
  sfor<8>([&](auto I) { constexpr unsigned p = I >> 2, v = I & 3, src = ((2 * p + 1) << 8) | (2 * p);
    sel[I] = ((c8 >> 1) == (int)v) ? ((c8 & 1) ? ((src << 16) | 0x0c0cu) : (0x0c0c0000u | src)) : 0x0c0c0c0cu; PIN(sel[I]); });
  { const v4u z = {0u, 0u, 0u, 0u};
    *(LAS v4u*)(L + L_Z + w * 2048 + lane * 16) = z; *(LAS v4u*)(L + L_Z + w * 2048 + 1024 + lane * 16) = z; }
  const int split = (xcd & 1) * 16 + j;
  const int skip = cb < 4 ? 3 : 7;
  const bool has2 = hh != skip;
  const int rb = g * 7 + hh - (hh > skip ? 1 : 0);
  const size_t kvoff = ((size_t)g * SK + (SK - WIN) + split * 128 + w * 32) * 256;
  const rsrc_t r1 = ISA ? rsrc((const char*)a.k + kvoff, 8192)
                        : rsrc((const char*)a.w1 + (size_t)(k1 * 256 + w * 64) * 7168 + col1 * 256, isF ? 63 * 7168 + 256 : 0);
  const rsrc_t rv = rsrc((const char*)a.v + kvoff, 8192);
  const rsrc_t ro = rsrc((const char*)a.wo + (size_t)(head * 128 + w * 32) * 2048 + cb * 256, 31 * 2048 + 256);
  const rsrc_t r2 = rsrc((const char*)a.w2 + (size_t)(rb * 128 + w * 32) * 2048 + cb * 256, has2 ? 31 * 2048 + 256 : 0);
  int v1 = ISA ? lane * 16 : kg * 16 * 7168 + n16 * 16;
  constexpr int s1 = ISA ? 1024 : 7168;
  int vo = kg * 2 * 2048 + n16 * 16, v2 = kg * 8 * 2048 + n16 * 16, vv = kg * 1024 + n16 * 16;
  unsigned a_qpub = WS_Q + (g * 4 + kq) * 4096 + colq * 256 + (lane & 31) * 8;
  unsigned a_og = WS_O + (head * 32 + kg * 8) * OREC + w * 128 + n16 * 8;
  unsigned a_ml = WS_O + (head * 32 + (lane >> 1)) * OREC + 512 + (lane & 1) * 8;
  unsigned a_hg = WS_H + kg * 16384 + (has2 ? rb : g * 7) * 512 + w * 128 + n16 * 8;
  unsigned a_opub = WS_OUT + (cb * 32 + head) * 512 + lane * 8;
  int l_red64w = L_RED + (w * 2 + (kg >> 1)) * 256 + c8 * 32 + (kg & 1) * 16, l_red64r = L_RED + (lane >> 5) * 1024 + (lane & 31) * 8;
  int l_red128w = w * 512 + n16 * 32 + (kg & 1) * 16, l_lane8 = lane * 8;
  int l_zw = L_Z + w * 2048 + 7 * 96 + n16 * 4 + (lane >> 5) * 2, l_zr = L_Z + w * 2048 + (7 - c8) * 96 + kg * 16;
  int l_hw = L_H + w * 64 + n16 * 4 + (lane >> 5) * 2, l_hr = L_H + w * 64 + kg * 16;
  PIN(v1); PIN(vo); PIN(v2); PIN(a_qpub); PIN(a_og); PIN(a_ml); PIN(a_hg); PIN(a_opub);
  PIN(l_red64w); PIN(l_red64r); PIN(l_red128w); PIN(l_lane8); PIN(l_zw); PIN(l_zr); PIN(l_hw); PIN(l_hr);
  SB();
  float mean_b, rden;
  {
    float xv[16];
    sfor<4>([&](auto I) { xv[2 * I] = lo2f(xa[(int)I]); xv[2 * I + 1] = hi2f(xa[(int)I]); xv[8 + 2 * I] = lo2f(xb[(int)I]); xv[9 + 2 * I] = hi2f(xb[(int)I]); });
    float s = xv[0];
    sfor<15>([&](auto I) { s += xv[I + 1]; });
    const float mean = wave_sum(s) * (1.f / 1024.f);
    float q = 0.f;
    sfor<16>([&](auto I) { const float d = xv[I] - mean; q = fmaf(d, d, q); });
    const float var = wave_sum(q) * (1.f / 1024.f);
    mean_b = rbf(mean);
    rden = __builtin_amdgcn_rcpf(rbf(__builtin_amdgcn_sqrtf(rbf(rbf(var) + 1e-5f))));
  }
  auto norm2 = [&](unsigned u) { const unsigned d = pk(lo2f(u) - mean_b, hi2f(u) - mean_b); return pk(lo2f(d) * rden, hi2f(d) * rden); };
  unsigned mq[4];
  sfor<4>([&](auto I) { mq[I] = norm2(xq[(int)I]); });
  asm volatile("" :: "v"(pf));
  STAMP(1);
  f32x4 acc = {0.f, 0.f, 0.f, 0.f};
  sfor<8>([&](auto I) { acc = mfma(onehot<I>(mq, sel), T0[I], acc); SB(); if (ISA || F_EARLY) T1[I] = ldw(r1, v1, I * s1); SB(); });
  STAMP(2);
  if ((kg >> 1) == (n16 >> 3)) *(LAS f32x4*)(L + l_red64w) = acc;
  LDS_BARRIER();
  if (w == 0) {
    f32x2 s = *(LAS f32x2*)(L + l_red64r);
    sfor<3>([&](auto I) { s += *(LAS f32x2*)(L + l_red64r + (I + 1) * 256); });
    const float s0 = x32_sum(s.x), s1_ = x32_sum(s.y);
    if (lane < 32) gst(ws, a_qpub, gran(s0, s1_, ta, tb));
  }
  SB();
  auto publish128 = [&](int lbuf, unsigned dst) {
    f32x2 s = *(LAS f32x2*)(L + lbuf + l_lane8);
    sfor<3>([&](auto I) { s += *(LAS f32x2*)(L + lbuf + l_lane8 + (I + 1) * 512); });
    gst(ws, dst, gran(s.x, s.y, ta, tb));
  };

  if constexpr (ISA) {
    LAS unsigned char* KL = L + L_K + w * 8192;
    sfor<8>([&](auto I) { const int key = 4 * I + kg;
      *(LAS v4u*)(KL + key * 256 + ((n16 ^ (key & 15)) * 16)) = T1[I]; SB();
      T2[I] = ldw(rv, vv, (I & 3) * 256 + (I >> 2) * 4096); SB(); });
    unsigned a_qg = WS_Q + g * 16384 + w * 1024 + lane * 8;
    u64 g8[8];
    sfor<8>([&](auto I) { g8[I] = gld(ws + (I >> 1) * 4096, a_qg + (I & 1) * 512); });
    SB();
    v4u KF[8], VF[8];
    sfor<8>([&](auto I) { KF[I] = *(LAS v4u*)(KL + ((I >> 2) * 16 + n16) * 256 + (((4 * (I & 3) + kg) ^ n16) * 16)); PIN(KF[I]); });
    int l_qw = L_Q + 2 * w * 272 + lane * 4, l_qr = L_Q + c8 * 272 + kg * 16, l_ow = L_O + (w * 8 + c8) * 528 + kg * 128, l_mlw = L_ML + c8 * 16 + w * 4;
    const int h2 = 2 * w + (lane >> 5);
    int l_mlr = L_ML + h2 * 16, l_or = L_O + h2 * 528 + (lane & 31) * 8;
    unsigned a_o = WS_O + ((g * 8 + h2) * 32 + split) * OREC + (lane & 31) * 8;
    PIN(l_qw); PIN(l_qr); PIN(l_ow); PIN(l_mlw); PIN(l_mlr); PIN(l_or); PIN(a_o);
    SB();
    tr8(T2, VF);
    sfor<8>([&](auto I) { PIN(VF[I]); });
    SB();
    v2u u8[8];
    for (unsigned spin = 0;; ++spin) {
      unsigned bad = 0;
      sfor<8>([&](auto I) { u8[I] = untag(g8[I], ta, tb); bad |= u8[I].x | u8[I].y; });
      if (__all((bad & 0xffu) == 0) || spin > SPIN_CAP) break;
      sfor<8>([&](auto I) { g8[I] = gld(ws + (I >> 1) * 4096, a_qg + (I & 1) * 512); });
    }
    STAMP(3);
    sfor<2>([&](auto E) {
      const f32x2 q2 = (__builtin_bit_cast(f32x2, u8[E]) + __builtin_bit_cast(f32x2, u8[2 + E])) + (__builtin_bit_cast(f32x2, u8[4 + E]) + __builtin_bit_cast(f32x2, u8[6 + E]));
      *(LAS unsigned*)(L + l_qw + E * 272) = pk(q2.x, q2.y); });
    SB();
    sfor<8>([&](auto I) { T0[I] = ldw(ro, vo, ((I & 3) * 8 + (I >> 2)) * 2048); });
    SB();
    LDS_BARRIER();
    f32x4 sa = {0.f, 0.f, 0.f, 0.f}, sb = {0.f, 0.f, 0.f, 0.f};
    sfor<4>([&](auto S) { const v4u qf = *(LAS v4u*)(L + l_qr + S * 64);
      sa = mfma(KF[S], qf, sa); sb = mfma(KF[4 + S], qf, sb); });
    float s[4];
    sfor<4>([&](auto I) { s[I] = n16 < 8 ? sa[(int)I] : sb[(int)I]; });
    sfor<2>([&](auto I) { const unsigned r = pk(s[2 * I], s[2 * I + 1]);
      const unsigned r2 = pk(lo2f(r) * 0.08838834764831845f, hi2f(r) * 0.08838834764831845f); s[2 * I] = lo2f(r2); s[2 * I + 1] = hi2f(r2); });
    float m = mx(mx(s[0], s[1]), mx(s[2], s[3]));
    m = mx(m, dpp<XOR8>(m));
    m = x32_max(x16_max(m));
    sfor<4>([&](auto I) { s[I] = ex2((s[I] - m) * LOG2E); });
    v4u p4;
    p4.x = pk(s[0], s[1]); p4.y = pk(s[2], s[3]); p4.z = dppu<XOR8>(p4.x); p4.w = dppu<XOR8>(p4.y);
    const f32x4 z = {0.f, 0.f, 0.f, 0.f};
    f32x4 o[8];
    sfor<8>([&](auto J) { o[J] = mfma(VF[J], p4, z); });
    float l = (s[0] + s[1]) + (s[2] + s[3]);
    l += dpp<XOR8>(l);
    l = x32_sum(x16_sum(l));
    if (n16 < 8) {
      sfor<8>([&](auto J) { *(LAS f32x4*)(L + l_ow + J * 16) = o[J]; });
      *(LAS float*)(L + l_mlw) = m; *(LAS float*)(L + l_mlw + 128) = l;
    }
    LDS_BARRIER();
    {
      const f32x4 m4 = *(LAS f32x4*)(L + l_mlr), l4 = *(LAS f32x4*)(L + l_mlr + 128);
      const float M = mx(mx(m4.x, m4.y), mx(m4.z, m4.w));
      float e[4];
      sfor<4>([&](auto U) { e[U] = ex2((m4[(int)U] - M) * LOG2E); });
      sfor<2>([&](auto E) {
        f32x2 oo = *(LAS f32x2*)(L + l_or + E * 256) * e[0];
        sfor<3>([&](auto U) { oo += *(LAS f32x2*)(L + l_or + (U + 1) * 8 * 528 + E * 256) * e[U + 1]; });
        gst(ws, a_o + E * 256, gran(oo.x, oo.y, ta, tb)); });
      const float ls = fmaf(l4.x, e[0], l4.y * e[1]) + fmaf(l4.z, e[2], l4.w * e[3]);
      if ((lane & 31) < 2) gst(ws, a_o + 512, ((u64)tag << 32) | f2u((lane & 1) ? ls : M));
    }
    STAMP(4);
  } else {
    if (!F_EARLY) { if (F_SLEEP) __builtin_amdgcn_s_sleep(F_SLEEP); sfor<8>([&](auto I) { T1[I] = ldw(r1, v1, I * s1); }); SB(); }
    unsigned m1[8];
    sfor<4>([&](auto I) { m1[I] = norm2(x1a[(int)I]); m1[4 + I] = norm2(x1b[(int)I]); });
    unsigned a_hpub = WS_H + k1 * 16384 + col1 * 512 + lane * 8;
    PIN(a_hpub);
    acc = f32x4{0.f, 0.f, 0.f, 0.f};
    sfor<8>([&](auto I) { acc = mfma(onehot<I>(m1, sel), T1[I], acc); SB(); T2[I] = ldw(r1, v1, (8 + I) * 7168); SB(); });
    sfor<8>([&](auto I) { acc = mfma(onehot<8 + I>(m1, sel), T2[I], acc); SB(); T0[I] = ldw(ro, vo, ((I & 3) * 8 + (I >> 2)) * 2048); SB(); });
    STAMP(3);
    if (lane < 32) *(LAS f32x4*)(L + L_RED2 + l_red128w) = acc;
    LDS_BARRIER();
    if (w == 0 && isF) publish128(L_RED2, a_hpub);
    STAMP(4);
  }
  SB();
  constexpr int GP = ISA ? GPA : GPF;
  sfor<GP>([&](auto I) { T1[I] = ldw(r2, v2, I * 2048); });
  SB();
  u64 go[8], gm, gh;
  auto sweep = [&]() { gm = gld(ws, a_ml); gh = gld(ws, a_hg); sfor<8>([&](auto I) { go[I] = gld(ws + (I >> 2) * 4 * OREC, a_og + (I & 3) * OREC); }); };
  sweep();
  SB();
  sfor<8 - GP>([&](auto I) { T1[GP + I] = ldw(r2, v2, (GP + I) * 2048); });
  SB();
  v4u WF[8];
  tr8(T0, WF);
  sfor<8>([&](auto I) { PIN(WF[I]); });
  SB();
  v2u uo[8], uh;
  auto check = [&]() { uh = untag(gh, ta, tb); unsigned bad = uh.x | uh.y; sfor<8>([&](auto I) { uo[I] = untag(go[I], ta, tb); bad |= uo[I].x | uo[I].y; });
    return __all((bad & 0xffu) == 0 && (unsigned)(gm >> 32) == tag); };
  if (!check()) {
    for (unsigned spin = 0; spin < SPIN_CAP; ++spin) {
      gm = gld(ws, a_ml); gh = gld(ws, a_hg);
      const v2u t = untag(gh, ta, tb);
      if (!__all(((t.x | t.y) & 0xffu) == 0 && (unsigned)(gm >> 32) == tag)) { if (POLL_SLEEP) __builtin_amdgcn_s_sleep(POLL_SLEEP); continue; }
      sweep();
      if (check()) break;
    }
  }
  STAMP(5);
  {
    const float v = u2f((unsigned)gm);
    const float mm = dpp<0xA0>(v), ll = dpp<0xF5>(v);
    float M = mx(mm, dpp<XOR2>(mm)); M = mx(M, dpp<XOR7>(M)); M = mx(M, dpp<XOR8>(M)); M = x32_max(x16_max(M));
    const float e = ex2((mm - M) * LOG2E);
    float o0 = 0.f, o1 = 0.f;
    sfor<8>([&](auto I) { const float wt = dpp<NEWB + 2 * I>(e); o0 = fmaf(wt, u2f(uo[I].x), o0); o1 = fmaf(wt, u2f(uo[I].y), o1); });
    float t = e * ll; t += dpp<XOR2>(t); t += dpp<XOR7>(t); t += dpp<XOR8>(t); t = x32_sum(x16_sum(t));
    const float oo = x16_sum(x32_sum2(o0, o1)) * __builtin_amdgcn_rcpf(t);
    *(LAS unsigned short*)(L + l_zw) = (unsigned short)pk(oo, oo);
    const float h = rbf(x16_sum(x32_sum2(u2f(uh.x), u2f(uh.y))));
    const float y = h * __builtin_amdgcn_rcpf(1.f + ex2(h * -LOG2E));
    *(LAS unsigned short*)(L + l_hw) = has2 ? (unsigned short)pk(y, y) : (unsigned short)0;
  }
  asm volatile("" ::: "memory");
  acc = f32x4{0.f, 0.f, 0.f, 0.f};
  const v4u mh4 = *(LAS v4u*)(L + l_hr);
  sfor<8>([&](auto J) { acc = mfma(*(LAS v4u*)(L + l_zr + J * 96), WF[J], acc); });
  const unsigned mh[4] = {mh4.x, mh4.y, mh4.z, mh4.w};
  sfor<8>([&](auto I) { acc = mfma(onehot<I>(mh, sel), T1[I], acc); });
  if (lane < 32) *(LAS f32x4*)(L + L_RED + l_red128w) = acc;
  LDS_BARRIER();
  if (w == 0) publish128(L_RED, a_opub);
  STAMP(6);
  if (!ISA && !isF) {
    const int sp = xcd * 2 + (j - 16), rcb = sp >> 1, e = (sp & 1) * 4 + w, col = rcb * 128 + e * 16 + c8 * 2;
    const unsigned xr = __builtin_amdgcn_raw_buffer_load_b32(rx, col * 2, 0, 0);
    const unsigned a_r = WS_OUT + rcb * 16384 + rg * 2048 + e * 64 + c8 * 8;
    v2u u4[4];
    for (unsigned spin = 0;; ++spin) {
      unsigned bad = 0;
      u64 g4[4];
      sfor<4>([&](auto I) { g4[I] = gld(ws, a_r + I * 512); });
      sfor<4>([&](auto I) { u4[I] = untag(g4[I], ta, tb); bad |= u4[I].x | u4[I].y; });
      if (__all((bad & 0xffu) == 0) || spin > SPIN_CAP) break;
    }
    const f32x2 s2 = (__builtin_bit_cast(f32x2, u4[0]) + __builtin_bit_cast(f32x2, u4[1])) + (__builtin_bit_cast(f32x2, u4[2]) + __builtin_bit_cast(f32x2, u4[3]));
    float s0 = s2.x, s1_ = s2.y;
    s0 += dpp<XOR8>(s0); s1_ += dpp<XOR8>(s1_);
    s0 = x32_sum(x16_sum(s0)); s1_ = x32_sum(x16_sum(s1_));
    if (lane < 8) *(GAS unsigned*)((GAS char*)a.out + col * 2) = pk(lo2f(xr) + s0, hi2f(xr) + s1_);
    if (sp == 0 && w == 0 && lane == 0) __hip_atomic_store((gu32*)(ws + WS_E), tag, RLX_AGENT);
    STAMP(7);
  }
}

extern "C" __global__ void __launch_bounds__(256, 2) fused_decode(const void* px, const void* pwq, void* pws, const void* pk, const void* pw1, const void* pv, const void* pwo, const void* pw2, void* pout
) {
  Args a; a.x = px; a.k = pk; a.v = pv; a.wq = pwq; a.wo = pwo; a.w1 = pw1; a.w2 = pw2; a.out = pout; a.ws = pws;
  __shared__ __attribute__((aligned(16))) unsigned char smem[L_END];
  LAS unsigned char* L = (LAS unsigned char*)smem;
  u64 ts[8] = {0, 0, 0, 0, 0, 0, 0, 0};
  asm volatile("s_memrealtime %0" : "=s"(ts[0]) :: "memory");
#define TS , ts
  unsigned pf = 0;
  if (PREF) {
    u64 pc; asm volatile("s_getpc_b64 %0" : "=s"(pc));
    pf = *(const GAS unsigned*)((pc & ~(u64)255) + (u64)(min((int)threadIdx.x >> 1, PF_LINES - 1) * 128));
  }
  SB();
  {
    asm volatile("s_memrealtime %0\n\ts_waitcnt lgkmcnt(0)" : "=s"(ts[1]) : "s"(px), "s"(pwq) : "memory");
    SB();
    unsigned xx = *(const GAS unsigned*)((const GAS char*)px + (threadIdx.x & 63) * 32);
    unsigned yy = __builtin_nontemporal_load((const GAS unsigned*)((const GAS char*)pwq + (size_t)blockIdx.x * 32768 + threadIdx.x * 128));
    SB();
    asm volatile("" : "+v"(xx));
    asm volatile("s_memrealtime %0\n\ts_waitcnt lgkmcnt(0)" : "=s"(ts[2]) :: "memory");
    SB();
    asm volatile("" : "+v"(yy));
    asm volatile("s_memrealtime %0\n\ts_waitcnt lgkmcnt(0)" : "=s"(ts[3]) :: "memory");
    SB();
  }
  if (blockIdx.x < 128) body<true>(a, L, pf TS); else body<false>(a, L, pf TS);
  { u64 q1 = (ts[2] - ts[0]) / 12, q2 = (ts[1] - ts[0]) / 12; if (q1 > 31) q1 = 31; if (q2 > 31) q2 = 31;
    const u64 until = ts[0] + 3000 + (q1 * 32 + q2) * 200;
    for (int it = 0; it < 10000000; ++it) { u64 t_; asm volatile("s_memrealtime %0\n\ts_waitcnt lgkmcnt(0)" : "=s"(t_) :: "memory"); if (t_ >= until) break; } }
}

#define STA
extern "C" void kernel_launch(void* const* d_in, const int* in_sizes, int n_in, void* d_out, int out_size, void* d_ws, size_t ws_size, hipStream_t stream) {
  hipLaunchKernelGGL(fused_decode, dim3(256), dim3(256), 0, stream, d_in[0], d_in[3], d_ws, d_in[1], d_in[6], d_in[2], d_in[4], d_in[7], d_out STA);
}
```

```cpp
#include <hip/hip_runtime.h>
#include <stdint.h>

struct Buf { const void* p; uint32_t bytes; uint32_t fmt; };
struct Args { Buf x, wq, ws, k, w1, v, wo, w2, out; };

__global__ void __launch_bounds__(256, 2) fused_decode(Args a) {
  __shared__ __attribute__((aligned(16))) unsigned char smem[66560];
  asm volatile(
    "s_load_dwordx16 s[4:19], %0, 0x0\n"
    "s_getpc_b64 s[66:67]\n"
    ".Lpc:\n"
    "v_lshrrev_b32 v7, 1, %2\n"
    "v_min_u32 v7, (.Lend-.Lpc)/128, v7\n"
    "v_lshlrev_b32 v7, 7, v7\n"
    "global_load_dword v39, v7, s[66:67]\n"
    "v_readfirstlane_b32 s49, %2\n"
    "v_and_b32 v1, 63, %2\n"
    "s_lshr_b32 s51, %1, 3\n"
    "s_and_b32 s68, %1, 1\n"
    "s_bfe_u32 s50, %1, 0x20001\n"
    "s_lshr_b32 s49, s49, 6\n"
    "s_and_b32 s56, s51, 28\n"
    "s_lshr_b32 s56, 0x73654210, s56\n"
    "s_and_b32 s56, s56, 7\n"
    "s_and_b32 s57, s51, 1\n"
    "s_lshl3_add_u32 s61, s57, s56\n"
    "s_bfe_u32 s56, s51, 0x10001\n"
    "s_lshl1_add_u32 s62, s68, s56\n"
    "s_lshl2_add_u32 s56, s62, s49\n"
    "s_lshl_b32 s56, s56, 6\n"
    "v_and_b32 v14, 56, %2\n"
    "v_and_b32 v4, 7, %2\n"
    "v_add_u32 v14, s56, v14\n"
    "s_xor_b32 s63, s50, 2\n"
    "s_lshl4_add_u32 s57, s63, s61\n"
    "s_lshl_b32 s57, s57, 7\n"
    "v_lshl_add_u32 v8, v4, 4, s57\n"
    "v_lshl_add_u32 v15, v14, 13, v8\n"
    "v_lshlrev_b32 v6, 4, v1\n"
    "s_lshr_b32 s56, s51, 4\n"
    "s_lshl1_add_u32 s56, s68, s56\n"
    "s_and_b32 s57, s51, 7\n"
    "s_lshl3_add_u32 s56, s56, s57\n"
    "s_lshl2_add_u32 s56, s56, s49\n"
    "s_lshl_b32 s56, s56, 16\n"
    "s_bfe_u32 s57, s51, 0x10003\n"
    "s_lshl1_add_u32 s57, s63, s57\n"
    "s_lshl_b32 s57, s57, 10\n"
    "s_add_u32 s56, s56, s57\n"
    "s_add_u32 s56, s56, 24960\n"
    "v_lshl_add_u32 v9, v1, 13, s56\n"
    "s_lshl_b32 s56, s68, 5\n"
    "s_add_u32 s56, s56, s51\n"
    "s_lshl2_add_u32 s56, s56, s49\n"
    "s_and_b32 s57, s56, 1\n"
    "s_lshr_b32 s56, s56, 1\n"
    "s_lshl2_add_u32 s56, s56, s57\n"
    "s_lshl_b32 s56, s56, 14\n"
    "s_add_u32 s56, s56, 0xffffff80\n"
    "v_cmp_gt_u32 vcc, 5, v1\n"
    "s_cmp_eq_u32 s63, 3\n"
    "s_cselect_b32 s56, s56, 2.0\n"
    "v_lshl_add_u32 v8, v1, 13, s56\n"
    "v_cndmask_b32 v9, v8, v9, vcc\n"
    "s_mov_b32 s40, 0x2000\n"
    "s_mov_b32 s41, 0x4000\n"
    "s_mov_b32 s42, 0x6000\n"
    "s_mov_b32 s43, 0x8000\n"
    "s_mov_b32 s44, 0xa000\n"
    "s_mov_b32 s45, 0xc000\n"
    "s_mov_b32 s46, 0xe000\n"
    "s_waitcnt lgkmcnt(0)\n"
    "buffer_load_dwordx4 v[184:187], v6, s[4:7], 0 offen\n"
    "buffer_load_dwordx4 v[188:191], v6, s[4:7], 0 offen offset:1024\n"
    "s_mov_b64 exec, 0x7f\n"
    "buffer_load_dword v39, v9, s[8:11], 0 offen\n"
    "s_mov_b64 exec, -1\n"
    "s_barrier\n"
    "buffer_load_dwordx4 v[80:83], v15, s[8:11], 0 offen nt\n"
    "s_mov_b64 exec, 1\n"
    "buffer_load_dword v37, off, s[12:15], 0 sc1\n"
    "s_mov_b64 exec, -1\n"
    "s_load_dwordx16 s[20:35], %0, 0x40\n"
    "s_load_dwordx4 s[36:39], %0, 0x80\n"
    "s_memrealtime s[70:71]\n"
    "s_lshl_b32 s58, s49, 13\n"
    "v_lshl_add_u32 v14, v14, 1, s58\n"
    "v_add_u32 v48, s58, v6\n"
    "v_lshrrev_b32 v2, 4, v1\n"
    "v_and_b32 v3, 15, %2\n"
    "buffer_load_dwordx4 v[84:87], v15, s[8:11], s40 offen nt\n"
    "v_lshlrev_b32 v5, 4, v3\n"
    "s_movk_i32 s47, 0x1000\n"
    "s_movk_i32 s48, 0x3000\n"
    "v_and_b32 v7, 1, %2\n"
    "v_lshrrev_b32 v8, 1, v4\n"
    "v_cmp_eq_u32 vcc, 0, v7\n"
    "v_mov_b32 v64, 0x01000c0c\n"
    "v_mov_b32 v65, 0x0c0c0100\n"
    "buffer_load_dwordx4 v[88:91], v15, s[8:11], s41 offen nt\n"
    "v_mov_b32 v66, 0x03020c0c\n"
    "v_mov_b32 v67, 0x0c0c0302\n"
    "v_mov_b32 v68, 0x0c0c0c0c\n"
    "v_cndmask_b32 v64, v64, v65, vcc\n"
    "v_cndmask_b32 v66, v66, v67, vcc\n"
    "v_cmp_eq_u32 vcc, 0, v8\n"
    "s_lshl2_add_u32 s56, s63, s62\n"
    "s_lshl4_add_u32 s56, s56, s61\n"
    "buffer_load_dwordx4 v[92:95], v15, s[8:11], s42 offen nt\n"
    "v_cndmask_b32 v40, v68, v64, vcc\n"
    "v_cndmask_b32 v44, v68, v66, vcc\n"
    "v_cmp_eq_u32 vcc, 1, v8\n"
    "s_lshl_b32 s56, s56, 8\n"
    "s_add_u32 s56, s56, 0x5000\n"
    "v_cndmask_b32 v41, v68, v64, vcc\n"
    "v_cndmask_b32 v45, v68, v66, vcc\n"
    "v_cmp_eq_u32 vcc, 2, v8\n"
    "buffer_load_dwordx4 v[96:99], v15, s[8:11], s43 offen nt\n"
    "v_lshl_add_u32 v21, v1, 2, s56\n"
    "s_lshl_b32 s57, s49, 9\n"
    "v_cndmask_b32 v42, v68, v64, vcc\n"
    "v_cndmask_b32 v46, v68, v66, vcc\n"
    "v_cmp_eq_u32 vcc, 3, v8\n"
    "v_lshl_add_u32 v7, v2, 3, v4\n"
    "v_lshl_add_u32 v29, v7, 4, s57\n"
    "v_cndmask_b32 v43, v68, v64, vcc\n"
    "buffer_load_dwordx4 v[100:103], v15, s[8:11], s44 offen nt\n"
    "v_cndmask_b32 v47, v68, v66, vcc\n"
    "v_lshl_add_u32 v30, v2, 9, v5\n"
    "v_lshrrev_b32 v10, 5, v1\n"
    "v_bfe_u32 v11, v1, 4, 1\n"
    "v_lshl_or_b32 v12, v11, 1, v10\n"
    "v_lshl_add_u32 v7, v3, 1, v11\n"
    "v_lshl_add_u32 v31, v7, 4, s57\n"
    "s_xor_b32 s56, %1, 4\n"
    "buffer_load_dwordx4 v[104:107], v15, s[8:11], s45 offen nt\n"
    "s_and_b32 s56, s56, 7\n"
    "s_and_b32 s57, s51, 3\n"
    "s_lshl2_add_u32 s69, s56, s57\n"
    "s_and_b32 s57, s51, 4\n"
    "s_lshr_b32 s56, s51, 3\n"
    "s_lshl_b32 s56, s56, 1\n"
    "s_lshr_b32 s56, 30, s56\n"
    "s_and_b32 s56, s56, 3\n"
    "buffer_load_dwordx4 v[108:111], v15, s[8:11], s46 offen nt\n"
    "s_or_b32 s64, s56, s57\n"
    "s_and_b32 s57, s49, 1\n"
    "s_lshl_b32 s56, s57, 8\n"
    "v_lshl_add_u32 v32, v1, 2, s56\n"
    "s_lshl1_add_u32 s57, s64, s57\n"
    "s_lshl_b32 s56, s57, 7\n"
    "v_lshl_add_u32 v26, v1, 1, s56\n"
    "v_add_u32 v27, s58, v26\n"
    "s_cmp_ge_u32 s51, 16\n"
    "s_cbranch_scc1 .Ltouch\n"
    ".Ltouch_back:\n"
    "s_waitcnt vmcnt(11)\n"
    "ds_write_b128 v48, v[184:187] offset:16896\n"
    "s_waitcnt vmcnt(10)\n"
    "ds_write_b128 v48, v[188:191] offset:17920\n"
    "ds_read_b128 v[192:195], v14 offset:16896\n"
    "ds_read_u16 v27, v27 offset:16896\n"
    "v_lshlrev_b32 v144, 16, v184\n"
    "v_and_b32 v145, 0xffff0000, v184\n"
    "v_lshlrev_b32 v146, 16, v185\n"
    "v_and_b32 v147, 0xffff0000, v185\n"
    "v_lshlrev_b32 v148, 16, v186\n"
    "v_and_b32 v149, 0xffff0000, v186\n"
    "v_lshlrev_b32 v150, 16, v187\n"
    "v_and_b32 v151, 0xffff0000, v187\n"
    "v_lshlrev_b32 v152, 16, v188\n"
    "v_and_b32 v153, 0xffff0000, v188\n"
    "v_lshlrev_b32 v154, 16, v189\n"
    "v_and_b32 v155, 0xffff0000, v189\n"
    "v_lshlrev_b32 v156, 16, v190\n"
    "v_and_b32 v157, 0xffff0000, v190\n"
    "v_lshlrev_b32 v158, 16, v191\n"
    "v_and_b32 v159, 0xffff0000, v191\n"
    "v_pk_add_f32 v[160:161], v[144:145], v[146:147]\n"
    "v_pk_add_f32 v[162:163], v[148:149], v[150:151]\n"
    "v_pk_add_f32 v[164:165], v[152:153], v[154:155]\n"
    "v_pk_add_f32 v[166:167], v[156:157], v[158:159]\n"
    "v_pk_add_f32 v[160:161], v[160:161], v[162:163]\n"
    "v_pk_add_f32 v[164:165], v[164:165], v[166:167]\n"
    "v_lshrrev_b32 v8, 3, v1\n"
    "v_pk_add_f32 v[160:161], v[160:161], v[164:165]\n"
    "v_lshlrev_b32 v9, 4, v4\n"
    "v_add_f32 v160, v160, v161\n"
    "s_lshl_b32 s56, s57, 10\n"
    "s_add_u32 s56, s56, 0x1000\n"
    "v_add_f32_dpp v160, v160, v160 quad_perm:[1,0,3,2] row_mask:0xf bank_mask:0xf bound_ctrl:1\n"
    "v_lshlrev_b32 v7, 3, v4\n"
    "v_lshl_add_u32 v7, v8, 7, v7\n"
    "v_add_f32_dpp v160, v160, v160 quad_perm:[2,3,0,1] row_mask:0xf bank_mask:0xf bound_ctrl:1\n"
    "v_add_u32 v25, s56, v7\n"
    "s_lshl_b32 s56, s64, 5\n"
    "v_add_f32_dpp v160, v160, v160 row_half_mirror row_mask:0xf bank_mask:0xf bound_ctrl:1\n"
    "s_add_u32 s56, s56, s69\n"
    "s_lshl_b32 s56, s56, 9\n"
    "v_add_f32_dpp v160, v160, v160 row_ror:8 row_mask:0xf bank_mask:0xf bound_ctrl:1\n"
    "v_mov_b32 v161, v160\n"
    "s_add_u32 s56, s56, 0x15000\n"
    "v_add_u32 v24, s56, v32\n"
    "v_permlane16_swap_b32 v160, v161\n"
    "v_add_f32 v160, v160, v161\n"
    "v_mov_b32 v161, v160\n"
    "s_lshl2_add_u32 s57, s69, s49\n"
    "v_lshl_add_u32 v7, v2, 11, v5\n"
    "v_permlane32_swap_b32 v160, v161\n"
    "v_add_f32 v160, v160, v161\n"
    "v_mul_f32 v160, 0x3a800000, v160\n"
    "v_pk_add_f32 v[144:145], v[144:145], v[160:161] op_sel_hi:[1,0] neg_lo:[0,1] neg_hi:[0,1]\n"
    "v_pk_add_f32 v[146:147], v[146:147], v[160:161] op_sel_hi:[1,0] neg_lo:[0,1] neg_hi:[0,1]\n"
    "v_pk_add_f32 v[148:149], v[148:149], v[160:161] op_sel_hi:[1,0] neg_lo:[0,1] neg_hi:[0,1]\n"
    "v_pk_add_f32 v[150:151], v[150:151], v[160:161] op_sel_hi:[1,0] neg_lo:[0,1] neg_hi:[0,1]\n"
    "v_pk_add_f32 v[152:153], v[152:153], v[160:161] op_sel_hi:[1,0] neg_lo:[0,1] neg_hi:[0,1]\n"
    "v_pk_add_f32 v[154:155], v[154:155], v[160:161] op_sel_hi:[1,0] neg_lo:[0,1] neg_hi:[0,1]\n"
    "v_pk_add_f32 v[156:157], v[156:157], v[160:161] op_sel_hi:[1,0] neg_lo:[0,1] neg_hi:[0,1]\n"
    "v_pk_add_f32 v[158:159], v[158:159], v[160:161] op_sel_hi:[1,0] neg_lo:[0,1] neg_hi:[0,1]\n"
    "v_cvt_pk_bf16_f32 v72, v160, v160\n"
    "v_pk_mul_f32 v[162:163], v[144:145], v[144:145]\n"
    "v_pk_mul_f32 v[164:165], v[146:147], v[146:147]\n"
    "v_pk_fma_f32 v[162:163], v[148:149], v[148:149], v[162:163]\n"
    "v_pk_fma_f32 v[164:165], v[150:151], v[150:151], v[164:165]\n"
    "v_pk_fma_f32 v[162:163], v[152:153], v[152:153], v[162:163]\n"
    "v_pk_fma_f32 v[164:165], v[154:155], v[154:155], v[164:165]\n"
    "v_pk_fma_f32 v[162:163], v[156:157], v[156:157], v[162:163]\n"
    "v_pk_fma_f32 v[164:165], v[158:159], v[158:159], v[164:165]\n"
    "v_lshlrev_b32 v72, 16, v72\n"
    "v_pk_add_f32 v[162:163], v[162:163], v[164:165]\n"
    "s_lshl_b32 s56, s57, 8\n"
    "v_add_f32 v74, v162, v163\n"
    "s_waitcnt lgkmcnt(0)\n"
    "v_lshlrev_b32 v144, 16, v192\n"
    "v_and_b32 v145, 0xffff0000, v192\n"
    "v_add_f32_dpp v74, v74, v74 quad_perm:[1,0,3,2] row_mask:0xf bank_mask:0xf bound_ctrl:1\n"
    "v_lshlrev_b32 v146, 16, v193\n"
    "v_and_b32 v147, 0xffff0000, v193\n"
    "v_add_f32_dpp v74, v74, v74 quad_perm:[2,3,0,1] row_mask:0xf bank_mask:0xf bound_ctrl:1\n"
    "v_lshlrev_b32 v148, 16, v194\n"
    "v_and_b32 v149, 0xffff0000, v194\n"
    "v_add_f32_dpp v74, v74, v74 row_half_mirror row_mask:0xf bank_mask:0xf bound_ctrl:1\n"
    "v_lshlrev_b32 v150, 16, v195\n"
    "v_and_b32 v151, 0xffff0000, v195\n"
    "v_add_f32_dpp v74, v74, v74 row_ror:8 row_mask:0xf bank_mask:0xf bound_ctrl:1\n"
    "v_mov_b32 v75, v74\n"
    "v_pk_add_f32 v[144:145], v[144:145], v[72:73] op_sel_hi:[1,0] neg_lo:[0,1] neg_hi:[0,1]\n"
    "v_pk_add_f32 v[146:147], v[146:147], v[72:73] op_sel_hi:[1,0] neg_lo:[0,1] neg_hi:[0,1]\n"
    "v_permlane16_swap_b32 v74, v75\n"
    "v_add_f32 v74, v74, v75\n"
    "v_mov_b32 v75, v74\n"
    "v_pk_add_f32 v[148:149], v[148:149], v[72:73] op_sel_hi:[1,0] neg_lo:[0,1] neg_hi:[0,1]\n"
    "v_pk_add_f32 v[150:151], v[150:151], v[72:73] op_sel_hi:[1,0] neg_lo:[0,1] neg_hi:[0,1]\n"
    "v_permlane32_swap_b32 v74, v75\n"
    "v_add_f32 v74, v74, v75\n"
    "v_mul_f32 v74, 0x3a800000, v74\n"
    "v_cvt_pk_bf16_f32 v74, v74, v74\n"
    "v_lshlrev_b32 v74, 16, v74\n"
    "v_add_f32 v74, 0x3727c5ac, v74\n"
    "v_cvt_pk_bf16_f32 v74, v74, v74\n"
    "v_lshlrev_b32 v74, 16, v74\n"
    "v_sqrt_f32 v74, v74\n"
    "v_cvt_pk_bf16_f32 v192, v144, v145\n"
    "v_cvt_pk_bf16_f32 v74, v74, v74\n"
    "v_lshlrev_b32 v74, 16, v74\n"
    "v_rcp_f32 v74, v74\n"
    "v_cvt_pk_bf16_f32 v193, v146, v147\n"
    "v_cvt_pk_bf16_f32 v194, v148, v149\n"
    "v_cvt_pk_bf16_f32 v195, v150, v151\n"
    "v_lshlrev_b32 v144, 16, v192\n"
    "v_and_b32 v145, 0xffff0000, v192\n"
    "v_lshlrev_b32 v146, 16, v193\n"
    "v_and_b32 v147, 0xffff0000, v193\n"
    "v_lshlrev_b32 v148, 16, v194\n"
    "v_and_b32 v149, 0xffff0000, v194\n"
    "v_lshlrev_b32 v150, 16, v195\n"
    "v_and_b32 v151, 0xffff0000, v195\n"
    "v_pk_mul_f32 v[144:145], v[144:145], v[74:75] op_sel_hi:[1,0]\n"
    "v_pk_mul_f32 v[146:147], v[146:147], v[74:75] op_sel_hi:[1,0]\n"
    "v_pk_mul_f32 v[148:149], v[148:149], v[74:75] op_sel_hi:[1,0]\n"
    "v_pk_mul_f32 v[150:151], v[150:151], v[74:75] op_sel_hi:[1,0]\n"
    "v_cvt_pk_bf16_f32 v64, v144, v145\n"
    "v_cvt_pk_bf16_f32 v65, v146, v147\n"
    "v_cvt_pk_bf16_f32 v66, v148, v149\n"
    "v_cvt_pk_bf16_f32 v67, v150, v151\n"
    "s_add_u32 s56, s56, s64\n"
    "s_lshl_b32 s56, s56, 8\n"
    "v_add_u32 v19, s56, v7\n"
    "s_mul_i32 s56, s57, 224\n"
    "s_add_u32 s56, s56, s64\n"
    "s_lshl_b32 s56, s56, 8\n"
    "v_add_u32 v20, s56, v7\n"
    "s_lshl_b32 s56, s57, 7\n"
    "s_add_u32 s56, s56, 0x35000\n"
    "v_lshl_add_u32 v7, v8, 15, v9\n"
    "v_add_u32 v23, s56, v7\n"
    "s_lshl_b32 s56, s69, 7\n"
    "s_add_u32 s56, s56, s49\n"
    "s_lshl_b32 s56, s56, 7\n"
    "s_add_u32 s56, s56, 0x75000\n"
    "v_lshl_add_u32 v7, v8, 11, v9\n"
    "v_add_u32 v22, s56, v7\n"
    "v_mov_b32 v208, 0\n"
    "v_mov_b32 v209, 0\n"
    "v_mov_b32 v210, 0\n"
    "v_mov_b32 v211, 0\n"
    "s_lshl_b32 s56, s49, 11\n"
    "v_add_u32 v7, s56, v6\n"
    "ds_write_b128 v7, v[208:211] offset:8704\n"
    "ds_write_b128 v7, v[208:211] offset:9728\n"
    "v_lshl_add_u32 v36, v2, 4, s56\n"
    "v_sub_u32 v7, 7, v4\n"
    "v_mul_u32_u24 v7, 96, v7\n"
    "v_add_u32 v34, v36, v7\n"
    "v_lshl_add_u32 v7, v12, 3, v4\n"
    "v_lshl_add_u32 v33, v7, 1, s56\n"
    "v_and_b32 v35, 3, v7\n"
    "v_lshrrev_b32 v7, 2, v7\n"
    "v_lshl_add_u32 v7, v35, 3, v7\n"
    "v_lshl_add_u32 v35, v7, 1, s56\n"
    "v_lshlrev_b32 v27, 16, v27\n"
    "s_mov_b32 s54, 0x200000\n"
    "s_mov_b32 s61, 0x05040100\n"
    "s_mov_b32 s62, 0x07060302\n"
    "s_mov_b32 s63, 0x05010400\n"
    "s_mov_b32 s72, 0\n"
    "s_mov_b32 s73, 0x41580000\n"
    "s_waitcnt vmcnt(7)\n"
    "v_readfirstlane_b32 s53, v37\n"
    "s_add_u32 s53, s53, 1\n"
    "s_and_b32 s52, s53, 15\n"
    "s_cselect_b32 s52, 0, 1\n"
    "s_add_u32 s53, s53, s52\n"
    "s_and_b32 s52, s53, 15\n"
    "s_mul_i32 s55, s52, 0x01010000\n"
    "v_mov_b32 v37, s53\n"
    "s_cmp_ge_u32 s51, 16\n"
    "s_cbranch_scc1 .Lffn\n"
    "s_lshl4_add_u32 s59, s68, s51\n"
    "s_lshl2_add_u32 s56, s59, s49\n"
    "s_lshl_b32 s56, s56, 13\n"
    "s_lshl_b32 s57, s50, 24\n"
    "s_add_u32 s56, s56, s57\n"
    "s_add_u32 s56, s56, 0xf00000\n"
    "v_add_u32 v17, s56, v6\n"
    "v_lshl_add_u32 v18, v2, 10, v5\n"
    "v_add_u32 v18, s56, v18\n"
    "v_xor_b32 v7, v3, v2\n"
    "v_lshl_add_u32 v7, v7, 4, s58\n"
    "v_lshl_add_u32 v48, v2, 8, v7\n"
    "v_lshl_add_u32 v52, v3, 8, v7\n"
    "v_xor_b32 v49, 64, v48\n"
    "v_xor_b32 v50, 128, v48\n"
    "v_xor_b32 v51, 192, v48\n"
    "v_perm_b32 v176, v64, v64, v40\n"
    "v_perm_b32 v177, v64, v64, v41\n"
    "v_perm_b32 v178, v64, v64, v42\n"
    "v_perm_b32 v179, v64, v64, v43\n"
    "buffer_load_dwordx4 v[112:115], v17, s[16:19], 0 offen nt\n"
    "s_nop 0\n"
    "v_mfma_f32_16x16x32_bf16 v[76:79], v[80:83], v[176:179], 0\n"
    "v_perm_b32 v180, v64, v64, v44\n"
    "v_perm_b32 v181, v64, v64, v45\n"
    "v_perm_b32 v182, v64, v64, v46\n"
    "v_perm_b32 v183, v64, v64, v47\n"
    "s_waitcnt vmcnt(7)\n"
    "buffer_load_dwordx4 v[116:119], v17, s[16:19], 0 offen offset:1024 nt\n"
    "v_mfma_f32_16x16x32_bf16 v[76:79], v[84:87], v[180:183], v[76:79]\n"
    "v_perm_b32 v176, v65, v65, v40\n"
    "v_perm_b32 v177, v65, v65, v41\n"
    "v_perm_b32 v178, v65, v65, v42\n"
    "v_perm_b32 v179, v65, v65, v43\n"
    "s_waitcnt vmcnt(7)\n"
    "buffer_load_dwordx4 v[120:123], v17, s[16:19], 0 offen offset:2048 nt\n"
    "v_mfma_f32_16x16x32_bf16 v[76:79], v[88:91], v[176:179], v[76:79]\n"
    "v_perm_b32 v180, v65, v65, v44\n"
    "v_perm_b32 v181, v65, v65, v45\n"
    "v_perm_b32 v182, v65, v65, v46\n"
    "v_perm_b32 v183, v65, v65, v47\n"
    "s_waitcnt vmcnt(7)\n"
    "buffer_load_dwordx4 v[124:127], v17, s[16:19], 0 offen offset:3072 nt\n"
    "v_mfma_f32_16x16x32_bf16 v[76:79], v[92:95], v[180:183], v[76:79]\n"
    "v_perm_b32 v176, v66, v66, v40\n"
    "v_perm_b32 v177, v66, v66, v41\n"
    "v_perm_b32 v178, v66, v66, v42\n"
    "v_perm_b32 v179, v66, v66, v43\n"
    "s_waitcnt vmcnt(7)\n"
    "buffer_load_dwordx4 v[128:131], v17, s[16:19], s47 offen nt\n"
    "v_mfma_f32_16x16x32_bf16 v[76:79], v[96:99], v[176:179], v[76:79]\n"
    "v_perm_b32 v180, v66, v66, v44\n"
    "v_perm_b32 v181, v66, v66, v45\n"
    "v_perm_b32 v182, v66, v66, v46\n"
    "v_perm_b32 v183, v66, v66, v47\n"
    "s_waitcnt vmcnt(7)\n"
    "buffer_load_dwordx4 v[132:135], v17, s[16:19], s47 offen offset:1024 nt\n"
    "v_mfma_f32_16x16x32_bf16 v[76:79], v[100:103], v[180:183], v[76:79]\n"
    "v_perm_b32 v176, v67, v67, v40\n"
    "v_perm_b32 v177, v67, v67, v41\n"
    "v_perm_b32 v178, v67, v67, v42\n"
    "v_perm_b32 v179, v67, v67, v43\n"
    "s_waitcnt vmcnt(7)\n"
    "buffer_load_dwordx4 v[136:139], v17, s[16:19], s47 offen offset:2048 nt\n"
    "v_mfma_f32_16x16x32_bf16 v[76:79], v[104:107], v[176:179], v[76:79]\n"
    "v_perm_b32 v180, v67, v67, v44\n"
    "v_perm_b32 v181, v67, v67, v45\n"
    "v_perm_b32 v182, v67, v67, v46\n"
    "v_perm_b32 v183, v67, v67, v47\n"
    "s_waitcnt vmcnt(7)\n"
    "buffer_load_dwordx4 v[140:143], v17, s[16:19], s47 offen offset:3072 nt\n"
    "v_mfma_f32_16x16x32_bf16 v[76:79], v[108:111], v[180:183], v[76:79]\n"
    "s_mov_b32 exec_lo, 0xff00ff\n"
    "s_mov_b32 exec_hi, 0xff00ff00\n"
    "s_cmp_lg_u32 s49, 0\n"
    "s_nop 4\n"
    "ds_write_b128 v29, v[76:79] offset:0\n"
    "s_mov_b64 exec, -1\n"
    "s_waitcnt lgkmcnt(0)\n"
    "s_barrier\n"
    "s_cbranch_scc1 .Lqp_a\n"
    "ds_read_b128 v[64:67], v30 offset:0\n"
    "ds_read_b128 v[68:71], v30 offset:256\n"
    "s_waitcnt lgkmcnt(0)\n"
    "v_pk_add_f32 v[64:65], v[64:65], v[68:69]\n"
    "v_pk_add_f32 v[66:67], v[66:67], v[70:71]\n"
    "s_nop 0\n"
    "v_permlane32_swap_b32 v64, v65\n"
    "v_permlane32_swap_b32 v66, v67\n"
    "v_add_f32 v64, v64, v65\n"
    "v_add_f32 v66, v66, v67\n"
    "s_nop 1\n"
    "v_permlane16_swap_b32 v64, v66\n"
    "v_add_f32 v64, v64, v66\n"
    "v_bfi_b32 v64, 15, v37, v64\n"
    "buffer_store_dword v64, v21, s[12:15], 0 offen sc1\n"
    ".Lqp_a:\n"
    "s_barrier\n"
    "s_waitcnt vmcnt(7)\n"
    "buffer_load_dwordx4 v[144:147], v18, s[24:27], 0 offen nt\n"
    "ds_write_b128 v48, v[112:115] offset:16896\n"
    "s_waitcnt vmcnt(7)\n"
    "buffer_load_dwordx4 v[148:151], v18, s[24:27], 0 offen offset:256 nt\n"
    "ds_write_b128 v49, v[116:119] offset:17920\n"
    "v_xor_b32 v53, 64, v52\n"
    "v_xor_b32 v54, 128, v52\n"
    "v_xor_b32 v55, 192, v52\n"
    "s_lshl4_add_u32 s57, s50, s49\n"
    "s_lshl_b32 s57, s57, 10\n"
    "s_add_u32 s57, s57, 0x5000\n"
    "v_add_u32 v56, s57, v6\n"
    "v_lshlrev_b32 v28, 7, v4\n"
    "s_waitcnt vmcnt(7)\n"
    "buffer_load_dwordx4 v[152:155], v18, s[24:27], 0 offen offset:512 nt\n"
    "ds_write_b128 v50, v[120:123] offset:18944\n"
    "s_lshl_b32 s57, s49, 1\n"
    "v_add_u32 v13, s57, v10\n"
    "v_mul_u32_u24 v57, 272, v13\n"
    "v_lshl_add_u32 v57, v11, 7, v57\n"
    "v_bfrev_b32 v7, v1\n"
    "v_bfe_u32 v7, v7, 28, 3\n"
    "v_lshl_add_u32 v57, v7, 4, v57\n"
    "v_and_b32 v7, 1, v1\n"
    "s_waitcnt vmcnt(7)\n"
    "buffer_load_dwordx4 v[156:159], v18, s[24:27], 0 offen offset:768 nt\n"
    "ds_write_b128 v51, v[124:127] offset:19968\n"
    "v_lshl_add_u32 v57, v7, 3, v57\n"
    "v_mul_u32_u24 v58, 272, v4\n"
    "v_lshl_add_u32 v58, v2, 4, v58\n"
    "s_mul_i32 s57, s49, 4224\n"
    "v_mul_u32_u24 v59, 528, v4\n"
    "v_and_b32 v7, 56, v1\n"
    "v_lshl_add_u32 v59, v7, 3, v59\n"
    "v_add_u32 v59, s57, v59\n"
    "s_waitcnt vmcnt(7)\n"
    "buffer_load_dwordx4 v[160:163], v18, s[24:27], s47 offen nt\n"
    "ds_write_b128 v48, v[128:131] offset:20992\n"
    "s_lshl_b32 s57, s49, 2\n"
    "s_add_u32 s57, s57, 8448\n"
    "v_lshl_add_u32 v60, v4, 4, s57\n"
    "v_lshlrev_b32 v61, 4, v13\n"
    "v_and_b32 v7, 0x1f0, v6\n"
    "v_mul_u32_u24 v62, 528, v13\n"
    "v_add_u32 v62, v62, v7\n"
    "s_lshl_b32 s57, s50, 8\n"
    "s_waitcnt vmcnt(7)\n"
    "buffer_load_dwordx4 v[164:167], v18, s[24:27], s47 offen offset:256 nt\n"
    "ds_write_b128 v49, v[132:135] offset:22016\n"
    "s_add_u32 s57, s57, s59\n"
    "s_lshl_b32 s57, s57, 9\n"
    "s_add_u32 s57, s57, 0x75000\n"
    "v_lshl_add_u32 v63, v13, 14, v7\n"
    "v_add_u32 v63, s57, v63\n"
    "s_lshl_b32 s56, s52, 8\n"
    "v_mov_b32 v38, s56\n"
    "s_movk_i32 s64, 0xfff\n"
    "s_waitcnt vmcnt(7)\n"
    "buffer_load_dwordx4 v[168:171], v18, s[24:27], s47 offen offset:512 nt\n"
    "ds_write_b128 v50, v[136:139] offset:23040\n"
    "s_mov_b32 s65, 0x3fb8aa3b\n"
    "s_mov_b32 s66, 0xc0c0500\n"
    "s_mov_b32 s67, 0xc0c0501\n"
    "s_mov_b32 s68, 0xc0c0502\n"
    "s_mov_b32 s69, 0xc0c0503\n"
    "v_mov_b32 v232, 0\n"
    "v_mov_b32 v233, 0\n"
    "v_mov_b32 v234, 0\n"
    "s_waitcnt vmcnt(7)\n"
    "buffer_load_dwordx4 v[172:175], v18, s[24:27], s47 offen offset:768 nt\n"
    "ds_write_b128 v51, v[140:143] offset:24064\n"
    "v_mov_b32 v235, 0\n"
    "v_mov_b32 v236, 0\n"
    "v_mov_b32 v237, 0\n"
    "v_mov_b32 v238, 0\n"
    "v_mov_b32 v239, 0\n"
    "ds_read_b128 v[112:115], v52 offset:16896\n"
    "ds_read_b128 v[116:119], v53 offset:16896\n"
    "ds_read_b128 v[120:123], v54 offset:16896\n"
    "ds_read_b128 v[124:127], v55 offset:16896\n"
    "ds_read_b128 v[128:131], v52 offset:20992\n"
    "ds_read_b128 v[132:135], v53 offset:20992\n"
    "ds_read_b128 v[136:139], v54 offset:20992\n"
    "ds_read_b128 v[140:143], v55 offset:20992\n"
    "s_waitcnt vmcnt(6)\n"
    "v_perm_b32 v176, v148, v144, s61\n"
    "v_perm_b32 v180, v148, v144, s62\n"
    "v_perm_b32 v184, v149, v145, s61\n"
    "v_perm_b32 v188, v149, v145, s62\n"
    "v_perm_b32 v192, v150, v146, s61\n"
    "v_perm_b32 v196, v150, v146, s62\n"
    "v_perm_b32 v200, v151, v147, s61\n"
    "v_perm_b32 v204, v151, v147, s62\n"
    "s_waitcnt vmcnt(4)\n"
    "s_barrier\n"
    "buffer_load_dword v224, v28, s[12:15], 0 offen offset:1024 sc1\n"
    "buffer_load_dwordx4 v[208:211], v56, s[12:15], 0 offen sc1\n"
    "buffer_load_dwordx4 v[212:215], v56, s[12:15], s47 offen sc1\n"
    "buffer_load_dwordx4 v[216:219], v56, s[12:15], s40 offen sc1\n"
    "buffer_load_dwordx4 v[220:223], v56, s[12:15], s48 offen sc1\n"
    "v_perm_b32 v177, v156, v152, s61\n"
    "v_perm_b32 v181, v156, v152, s62\n"
    "v_perm_b32 v185, v157, v153, s61\n"
    "v_perm_b32 v189, v157, v153, s62\n"
    "v_perm_b32 v193, v158, v154, s61\n"
    "v_perm_b32 v197, v158, v154, s62\n"
    "v_perm_b32 v201, v159, v155, s61\n"
    "v_perm_b32 v205, v159, v155, s62\n"
    "buffer_load_dwordx4 v[80:83], v19, s[28:31], 0 offen nt\n"
    "buffer_load_dwordx4 v[84:87], v19, s[28:31], s40 offen nt\n"
    "buffer_load_dwordx4 v[88:91], v19, s[28:31], s41 offen nt\n"
    "buffer_load_dwordx4 v[92:95], v19, s[28:31], s42 offen nt\n"
    "s_waitcnt vmcnt(11)\n"
    "v_perm_b32 v178, v164, v160, s61\n"
    "v_perm_b32 v182, v164, v160, s62\n"
    "v_perm_b32 v186, v165, v161, s61\n"
    "v_perm_b32 v190, v165, v161, s62\n"
    "v_perm_b32 v194, v166, v162, s61\n"
    "v_perm_b32 v198, v166, v162, s62\n"
    "v_perm_b32 v202, v167, v163, s61\n"
    "v_perm_b32 v206, v167, v163, s62\n"
    "buffer_load_dwordx4 v[96:99], v19, s[28:31], s43 offen nt\n"
    "buffer_load_dwordx4 v[100:103], v19, s[28:31], s44 offen nt\n"
    "s_waitcnt vmcnt(11)\n"
    "v_perm_b32 v179, v172, v168, s61\n"
    "v_perm_b32 v183, v172, v168, s62\n"
    "v_perm_b32 v187, v173, v169, s61\n"
    "v_perm_b32 v191, v173, v169, s62\n"
    "v_perm_b32 v195, v174, v170, s61\n"
    "v_perm_b32 v199, v174, v170, s62\n"
    "v_perm_b32 v203, v175, v171, s61\n"
    "v_perm_b32 v207, v175, v171, s62\n"
    "s_branch .Lchk_q\n"
    ".Lretry_q:\n"
    "s_sub_u32 s54, s54, 1\n"
    "s_cselect_b32 s54, 0, s54\n"
    "s_cbranch_scc1 .Lcont_q\n"
    "buffer_load_dword v224, v28, s[12:15], 0 offen offset:1024 sc1\n"
    "buffer_load_dwordx4 v[208:211], v56, s[12:15], 0 offen sc1\n"
    "buffer_load_dwordx4 v[212:215], v56, s[12:15], s47 offen sc1\n"
    "buffer_load_dwordx4 v[216:219], v56, s[12:15], s40 offen sc1\n"
    "buffer_load_dwordx4 v[220:223], v56, s[12:15], s48 offen sc1\n"
    "s_waitcnt vmcnt(0)\n"
    ".Lchk_q:\n"
    "s_waitcnt vmcnt(9)\n"
    "v_xor_b32 v208, s52, v208\n"
    "v_xor_b32 v209, s52, v209\n"
    "v_xor_b32 v210, s52, v210\n"
    "v_or3_b32 v64, v208, v209, v210\n"
    "v_xor_b32 v211, s52, v211\n"
    "s_waitcnt vmcnt(8)\n"
    "v_xor_b32 v212, s52, v212\n"
    "v_or3_b32 v64, v64, v211, v212\n"
    "v_xor_b32 v213, s52, v213\n"
    "v_xor_b32 v214, s52, v214\n"
    "v_or3_b32 v64, v64, v213, v214\n"
    "v_xor_b32 v215, s52, v215\n"
    "s_waitcnt vmcnt(7)\n"
    "v_xor_b32 v216, s52, v216\n"
    "v_or3_b32 v64, v64, v215, v216\n"
    "v_xor_b32 v217, s52, v217\n"
    "v_xor_b32 v218, s52, v218\n"
    "v_or3_b32 v64, v64, v217, v218\n"
    "v_xor_b32 v219, s52, v219\n"
    "s_waitcnt vmcnt(6)\n"
    "v_xor_b32 v220, s52, v220\n"
    "v_or3_b32 v64, v64, v219, v220\n"
    "v_xor_b32 v221, s52, v221\n"
    "v_xor_b32 v222, s52, v222\n"
    "v_or3_b32 v64, v64, v221, v222\n"
    "v_xor_b32 v223, s52, v223\n"
    "v_bitop3_b32 v64, v64, v223, 15 bitop3:0xa8\n"
    "v_bitop3_b32 v64, v64, v224, s53 bitop3:0xf6\n"
    "v_cmp_ne_u32 vcc, 0, v64\n"
    "s_cbranch_vccnz .Lretry_q\n"
    ".Lcont_q:\n"
    "v_pk_add_f32 v[208:209], v[208:209], v[212:213]\n"
    "v_pk_add_f32 v[210:211], v[210:211], v[214:215]\n"
    "v_pk_add_f32 v[216:217], v[216:217], v[220:221]\n"
    "v_pk_add_f32 v[218:219], v[218:219], v[222:223]\n"
    "v_pk_add_f32 v[208:209], v[208:209], v[216:217]\n"
    "v_pk_add_f32 v[210:211], v[210:211], v[218:219]\n"
    "v_cvt_pk_bf16_f32 v208, v208, v209\n"
    "v_cvt_pk_bf16_f32 v209, v210, v211\n"
    "ds_write_b64 v57, v[208:209] offset:6144\n"
    "s_waitcnt lgkmcnt(0)\n"
    "s_barrier\n"
    "ds_read_b128 v[208:211], v58 offset:6144\n"
    "ds_read_b128 v[212:215], v58 offset:6208\n"
    "ds_read_b128 v[216:219], v58 offset:6272\n"
    "ds_read_b128 v[220:223], v58 offset:6336\n"
    "v_cmp_gt_u32 vcc, 8, v3\n"
    "s_waitcnt lgkmcnt(3)\n"
    "v_mfma_f32_16x16x32_bf16 v[64:67], v[112:115], v[208:211], 0\n"
    "v_mfma_f32_16x16x32_bf16 v[68:71], v[128:131], v[208:211], 0\n"
    "s_waitcnt lgkmcnt(2)\n"
    "v_mfma_f32_16x16x32_bf16 v[64:67], v[116:119], v[212:215], v[64:67]\n"
    "v_mfma_f32_16x16x32_bf16 v[68:71], v[132:135], v[212:215], v[68:71]\n"
    "s_waitcnt lgkmcnt(1)\n"
    "v_mfma_f32_16x16x32_bf16 v[64:67], v[120:123], v[216:219], v[64:67]\n"
    "v_mfma_f32_16x16x32_bf16 v[68:71], v[136:139], v[216:219], v[68:71]\n"
    "s_waitcnt lgkmcnt(0)\n"
    "v_mfma_f32_16x16x32_bf16 v[64:67], v[124:127], v[220:223], v[64:67]\n"
    "v_mfma_f32_16x16x32_bf16 v[68:71], v[140:143], v[220:223], v[68:71]\n"
    "s_nop 7\n"
    "v_cndmask_b32 v208, v68, v64, vcc\n"
    "v_cndmask_b32 v209, v69, v65, vcc\n"
    "v_cndmask_b32 v210, v70, v66, vcc\n"
    "v_cndmask_b32 v211, v71, v67, vcc\n"
    "v_cvt_pk_bf16_f32 v212, v208, v209\n"
    "v_cvt_pk_bf16_f32 v213, v210, v211\n"
    "v_lshlrev_b32 v208, 16, v212\n"
    "v_and_b32 v209, 0xffff0000, v212\n"
    "v_lshlrev_b32 v210, 16, v213\n"
    "v_and_b32 v211, 0xffff0000, v213\n"
    "v_mul_f32 v208, 0x3db504f3, v208\n"
    "v_mul_f32 v209, 0x3db504f3, v209\n"
    "v_mul_f32 v210, 0x3db504f3, v210\n"
    "v_mul_f32 v211, 0x3db504f3, v211\n"
    "v_cvt_pk_bf16_f32 v212, v208, v209\n"
    "v_cvt_pk_bf16_f32 v213, v210, v211\n"
    "v_lshlrev_b32 v208, 16, v212\n"
    "v_and_b32 v209, 0xffff0000, v212\n"
    "v_lshlrev_b32 v210, 16, v213\n"
    "v_and_b32 v211, 0xffff0000, v213\n"
    "v_max3_f32 v216, v208, v209, v210\n"
    "v_max_f32 v216, v216, v211\n"
    "s_nop 1\n"
    "v_max_f32_dpp v216, v216, v216 row_ror:8 row_mask:0xf bank_mask:0xf bound_ctrl:1\n"
    "v_mov_b32 v219, v216\n"
    "s_nop 1\n"
    "v_permlane16_swap_b32 v216, v219\n"
    "v_max_f32 v216, v216, v219\n"
    "v_mov_b32 v219, v216\n"
    "s_nop 1\n"
    "v_permlane32_swap_b32 v216, v219\n"
    "v_max_f32 v216, v216, v219\n"
    "v_pk_add_f32 v[208:209], v[208:209], v[216:217] op_sel_hi:[1,0] neg_lo:[0,1] neg_hi:[0,1]\n"
    "v_pk_add_f32 v[210:211], v[210:211], v[216:217] op_sel_hi:[1,0] neg_lo:[0,1] neg_hi:[0,1]\n"
    "v_mul_f32 v208, s65, v208\n"
    "v_mul_f32 v209, s65, v209\n"
    "v_mul_f32 v210, s65, v210\n"
    "v_mul_f32 v211, s65, v211\n"
    "v_exp_f32 v208, v208\n"
    "v_exp_f32 v209, v209\n"
    "v_exp_f32 v210, v210\n"
    "v_exp_f32 v211, v211\n"
    "v_cvt_pk_bf16_f32 v212, v208, v209\n"
    "v_cvt_pk_bf16_f32 v213, v210, v211\n"
    "v_add_f32 v218, v208, v209\n"
    "v_mov_b32_dpp v232, v212 quad_perm:[0,1,2,3] row_mask:0xf bank_mask:0x3\n"
    "v_mov_b32_dpp v233, v213 quad_perm:[0,1,2,3] row_mask:0xf bank_mask:0x3\n"
    "v_mov_b32_dpp v234, v212 row_ror:8 row_mask:0xf bank_mask:0x3\n"
    "v_mov_b32_dpp v235, v213 row_ror:8 row_mask:0xf bank_mask:0x3\n"
    "v_mov_b32_dpp v236, v212 row_ror:8 row_mask:0xf bank_mask:0xc\n"
    "v_mov_b32_dpp v237, v213 row_ror:8 row_mask:0xf bank_mask:0xc\n"
    "v_mfma_f32_16x16x32_bf16 v[112:115], v[176:179], v[232:235], 0\n"
    "v_mov_b32_dpp v238, v212 quad_perm:[0,1,2,3] row_mask:0xf bank_mask:0xc\n"
    "v_mfma_f32_16x16x32_bf16 v[116:119], v[180:183], v[232:235], 0\n"
    "v_mov_b32_dpp v239, v213 quad_perm:[0,1,2,3] row_mask:0xf bank_mask:0xc\n"
    "v_mfma_f32_16x16x32_bf16 v[120:123], v[184:187], v[232:235], 0\n"
    "v_add_f32 v219, v210, v211\n"
    "v_add_f32 v218, v218, v219\n"
    "v_mfma_f32_16x16x32_bf16 v[124:127], v[188:191], v[232:235], 0\n"
    "v_mfma_f32_16x16x32_bf16 v[112:115], v[192:195], v[236:239], v[112:115]\n"
    "v_add_f32_dpp v218, v218, v218 row_ror:8 row_mask:0xf bank_mask:0xf bound_ctrl:1\n"
    "v_mov_b32 v219, v218\n"
    "v_mfma_f32_16x16x32_bf16 v[116:119], v[196:199], v[236:239], v[116:119]\n"
    "v_mfma_f32_16x16x32_bf16 v[120:123], v[200:203], v[236:239], v[120:123]\n"
    "v_permlane16_swap_b32 v218, v219\n"
    "v_add_f32 v218, v218, v219\n"
    "v_mov_b32 v219, v218\n"
    "v_mfma_f32_16x16x32_bf16 v[124:127], v[204:207], v[236:239], v[124:127]\n"
    "s_nop 0\n"
    "v_permlane32_swap_b32 v218, v219\n"
    "v_add_f32 v218, v218, v219\n"
    "ds_write_b128 v59, v[112:115] offset:49664\n"
    "ds_write_b128 v59, v[116:119] offset:49680\n"
    "ds_write2_b32 v60, v216, v218 offset1:32\n"
    "ds_write_b128 v59, v[120:123] offset:49696\n"
    "s_nop 0\n"
    "ds_write_b128 v59, v[124:127] offset:49712\n"
    "s_waitcnt lgkmcnt(0)\n"
    "s_barrier\n"
    "ds_read_b128 v[64:67], v61 offset:8448\n"
    "ds_read_b128 v[68:71], v61 offset:8576\n"
    "ds_read_b128 v[208:211], v62 offset:49664\n"
    "ds_read_b128 v[212:215], v62 offset:53888\n"
    "ds_read_b128 v[216:219], v62 offset:58112\n"
    "ds_read_b128 v[220:223], v62 offset:62336\n"
    "s_waitcnt lgkmcnt(5)\n"
    "v_max3_f32 v224, v64, v65, v66\n"
    "v_max_f32 v224, v224, v67\n"
    "v_pk_add_f32 v[64:65], v[64:65], v[224:225] op_sel_hi:[1,0] neg_lo:[0,1] neg_hi:[0,1]\n"
    "v_pk_add_f32 v[66:67], v[66:67], v[224:225] op_sel_hi:[1,0] neg_lo:[0,1] neg_hi:[0,1]\n"
    "v_mul_f32 v64, s65, v64\n"
    "v_mul_f32 v65, s65, v65\n"
    "v_mul_f32 v66, s65, v66\n"
    "v_mul_f32 v67, s65, v67\n"
    "v_exp_f32 v64, v64\n"
    "v_exp_f32 v65, v65\n"
    "v_exp_f32 v66, v66\n"
    "v_exp_f32 v67, v67\n"
    "s_waitcnt lgkmcnt(4)\n"
    "v_pk_mul_f32 v[68:69], v[68:69], v[64:65]\n"
    "s_waitcnt lgkmcnt(3)\n"
    "v_pk_mul_f32 v[208:209], v[208:209], v[64:65] op_sel_hi:[1,0]\n"
    "v_pk_mul_f32 v[210:211], v[210:211], v[64:65] op_sel_hi:[1,0]\n"
    "v_pk_fma_f32 v[68:69], v[70:71], v[66:67], v[68:69]\n"
    "s_waitcnt lgkmcnt(2)\n"
    "v_pk_fma_f32 v[208:209], v[212:213], v[64:65], v[208:209] op_sel:[0,1,0]\n"
    "v_pk_fma_f32 v[210:211], v[214:215], v[64:65], v[210:211] op_sel:[0,1,0]\n"
    "v_add_f32 v68, v68, v69\n"
    "v_log_f32 v225, v68\n"
    "v_rcp_f32 v226, v68\n"
    "s_waitcnt lgkmcnt(1)\n"
    "v_pk_fma_f32 v[208:209], v[216:217], v[66:67], v[208:209] op_sel_hi:[1,0,1]\n"
    "v_pk_fma_f32 v[210:211], v[218:219], v[66:67], v[210:211] op_sel_hi:[1,0,1]\n"
    "v_fma_f32 v224, v224, s65, v225\n"
    "s_waitcnt lgkmcnt(0)\n"
    "v_pk_fma_f32 v[208:209], v[220:221], v[66:67], v[208:209] op_sel:[0,1,0]\n"
    "v_pk_fma_f32 v[210:211], v[222:223], v[66:67], v[210:211] op_sel:[0,1,0]\n"
    "v_perm_b32 v68, v38, v224, s66\n"
    "v_perm_b32 v69, v38, v224, s67\n"
    "v_pk_mul_f32 v[208:209], v[208:209], v[226:227] op_sel_hi:[1,0]\n"
    "v_pk_mul_f32 v[210:211], v[210:211], v[226:227] op_sel_hi:[1,0]\n"
    "v_perm_b32 v70, v38, v224, s68\n"
    "v_perm_b32 v71, v38, v224, s69\n"
    "v_bfi_b32 v208, s64, v68, v208\n"
    "v_bfi_b32 v209, s64, v69, v209\n"
    "v_bfi_b32 v210, s64, v70, v210\n"
    "v_bfi_b32 v211, s64, v71, v211\n"
    "buffer_store_dwordx4 v[208:211], v63, s[12:15], 0 offen sc1\n"
    "s_barrier\n"
    "buffer_load_dwordx4 v[104:107], v19, s[28:31], s45 offen nt\n"
    "buffer_load_dwordx4 v[108:111], v19, s[28:31], s46 offen nt\n"
    "s_waitcnt vmcnt(7)\n"
    "v_perm_b32 v144, v84, v80, s61\n"
    "v_perm_b32 v148, v84, v80, s62\n"
    "v_perm_b32 v152, v85, v81, s61\n"
    "v_perm_b32 v156, v85, v81, s62\n"
    "v_perm_b32 v160, v86, v82, s61\n"
    "v_perm_b32 v164, v86, v82, s62\n"
    "v_perm_b32 v168, v87, v83, s61\n"
    "v_perm_b32 v172, v87, v83, s62\n"
    "buffer_load_dwordx4 v[112:115], v20, s[32:35], 0 offen nt\n"
    "buffer_load_dwordx4 v[116:119], v20, s[32:35], s40 offen nt\n"
    "s_waitcnt vmcnt(7)\n"
    "v_perm_b32 v145, v92, v88, s61\n"
    "v_perm_b32 v149, v92, v88, s62\n"
    "v_perm_b32 v153, v93, v89, s61\n"
    "v_perm_b32 v157, v93, v89, s62\n"
    "v_perm_b32 v161, v94, v90, s61\n"
    "v_perm_b32 v165, v94, v90, s62\n"
    "v_perm_b32 v169, v95, v91, s61\n"
    "v_perm_b32 v173, v95, v91, s62\n"
    "buffer_load_dwordx4 v[120:123], v20, s[32:35], s41 offen nt\n"
    "buffer_load_dwordx4 v[124:127], v20, s[32:35], s42 offen nt\n"
    ".Lgatea:\n"
    "s_memrealtime s[56:57]\n"
    "s_waitcnt lgkmcnt(0)\n"
    "s_sub_u32 s56, s56, s70\n"
    "s_cmp_lt_u32 s56, 550\n"
    "s_cbranch_scc1 .Lgatea\n"
    "buffer_load_dwordx4 v[224:227], v23, s[12:15], 0 offen sc1\n"
    "buffer_load_dwordx4 v[228:231], v23, s[12:15], s41 offen sc1\n"
    "s_barrier\n"
    "buffer_load_dwordx4 v[208:211], v22, s[12:15], 0 offen sc1\n"
    "buffer_load_dwordx4 v[212:215], v22, s[12:15], 0 offen offset:512 sc1\n"
    "buffer_load_dwordx4 v[216:219], v22, s[12:15], 0 offen offset:1024 sc1\n"
    "buffer_load_dwordx4 v[220:223], v22, s[12:15], 0 offen offset:1536 sc1\n"
    "s_barrier\n"
    "buffer_load_dwordx4 v[128:131], v20, s[32:35], s43 offen nt\n"
    "buffer_load_dwordx4 v[132:135], v20, s[32:35], s44 offen nt\n"
    "buffer_load_dwordx4 v[136:139], v20, s[32:35], s45 offen nt\n"
    "v_cvt_f64_f32 v[232:233], v27\n"
    "s_mov_b32 s56, 0\n"
    "s_mov_b32 s57, 0xc1502000\n"
    "v_add_f64 v[232:233], v[232:233], s[56:57]\n"
    "v_mov_b32 v234, 0xbea84000\n"
    "v_mov_b32 v235, 0xbeb84000\n"
    "v_mov_b32 v236, 0x4004000\n"
    "v_mov_b32 v237, 0x41500000\n"
    "s_waitcnt vmcnt(16)\n"
    "v_perm_b32 v146, v100, v96, s61\n"
    "v_perm_b32 v150, v100, v96, s62\n"
    "v_perm_b32 v154, v101, v97, s61\n"
    "v_perm_b32 v158, v101, v97, s62\n"
    "v_perm_b32 v162, v102, v98, s61\n"
    "v_perm_b32 v166, v102, v98, s62\n"
    "v_perm_b32 v170, v103, v99, s61\n"
    "v_perm_b32 v174, v103, v99, s62\n"
    "s_waitcnt vmcnt(13)\n"
    "v_perm_b32 v147, v108, v104, s61\n"
    "v_perm_b32 v151, v108, v104, s62\n"
    "v_perm_b32 v155, v109, v105, s61\n"
    "v_perm_b32 v159, v109, v105, s62\n"
    "v_perm_b32 v163, v110, v106, s61\n"
    "v_perm_b32 v167, v110, v106, s62\n"
    "v_perm_b32 v171, v111, v107, s61\n"
    "v_perm_b32 v175, v111, v107, s62\n"
    "s_branch .Lchk_ha\n"
    ".Lretry_ha:\n"
    "s_sub_u32 s54, s54, 1\n"
    "s_cselect_b32 s54, 0, s54\n"
    "s_cbranch_scc1 .Lcont_ha\n"
    "buffer_load_dwordx4 v[224:227], v23, s[12:15], 0 offen sc1\n"
    "buffer_load_dwordx4 v[228:231], v23, s[12:15], s41 offen sc1\n"
    "s_waitcnt vmcnt(0)\n"
    ".Lchk_ha:\n"
    "s_waitcnt vmcnt(8)\n"
    "v_xor_b32 v224, s52, v224\n"
    "v_xor_b32 v225, s52, v225\n"
    "v_xor_b32 v226, s52, v226\n"
    "v_or3_b32 v68, v224, v225, v226\n"
    "v_xor_b32 v227, s52, v227\n"
    "s_waitcnt vmcnt(7)\n"
    "v_xor_b32 v228, s52, v228\n"
    "v_or3_b32 v68, v68, v227, v228\n"
    "v_xor_b32 v229, s52, v229\n"
    "v_xor_b32 v230, s52, v230\n"
    "v_or3_b32 v68, v68, v229, v230\n"
    "v_xor_b32 v231, s52, v231\n"
    "v_bitop3_b32 v68, v68, v231, 15 bitop3:0xa8\n"
    "v_cmp_ne_u32 vcc, 0, v68\n"
    "s_cbranch_vccnz .Lretry_ha\n"
    ".Lcont_ha:\n"
    "v_pk_add_f32 v[224:225], v[224:225], v[228:229]\n"
    "v_pk_add_f32 v[226:227], v[226:227], v[230:231]\n"
    "s_nop 0\n"
    "v_permlane32_swap_b32 v224, v225\n"
    "v_permlane32_swap_b32 v226, v227\n"
    "v_add_f32 v224, v224, v225\n"
    "v_add_f32 v226, v226, v227\n"
    "s_nop 1\n"
    "v_permlane16_swap_b32 v224, v226\n"
    "v_add_f32 v224, v224, v226\n"
    "s_nop 1\n"
    "v_add_f32_dpp v224, v224, v224 row_ror:8 row_mask:0xf bank_mask:0xf bound_ctrl:1\n"
    "v_cvt_pk_bf16_f32 v224, v224, v224\n"
    "v_lshlrev_b32 v224, 16, v224\n"
    "v_mul_f32 v225, 0xbfb8aa3b, v224\n"
    "v_exp_f32 v225, v225\n"
    "s_nop 0\n"
    "v_add_f32 v225, 1.0, v225\n"
    "v_rcp_f32 v225, v225\n"
    "s_nop 0\n"
    "v_mul_f32 v224, v224, v225\n"
    "v_cvt_pk_bf16_f32 v224, v224, v224\n"
    "ds_write_b16 v33, v224 offset:10240\n"
    "ds_read_b128 v[228:231], v36 offset:10240\n"
    "s_branch .Lchk_oa\n"
    ".Lretry_oa:\n"
    "s_sub_u32 s54, s54, 1\n"
    "s_cselect_b32 s54, 0, s54\n"
    "s_cbranch_scc1 .Lcont_oa\n"
    "buffer_load_dwordx4 v[208:211], v22, s[12:15], 0 offen sc1\n"
    "buffer_load_dwordx4 v[212:215], v22, s[12:15], 0 offen offset:512 sc1\n"
    "buffer_load_dwordx4 v[216:219], v22, s[12:15], 0 offen offset:1024 sc1\n"
    "buffer_load_dwordx4 v[220:223], v22, s[12:15], 0 offen offset:1536 sc1\n"
    "s_waitcnt vmcnt(0)\n"
    ".Lchk_oa:\n"
    "s_waitcnt vmcnt(6)\n"
    "v_perm_b32 v69, v209, v208, s63\n"
    "v_perm_b32 v70, v211, v210, s63\n"
    "v_xor_b32 v68, s55, v69\n"
    "v_bitop3_b32 v68, v68, v70, s55 bitop3:0xf6\n"
    "v_perm_b32 v64, v70, v69, s61\n"
    "v_and_b32 v208, 0xfffff000, v208\n"
    "v_and_b32 v209, 0xfffff000, v209\n"
    "v_and_b32 v210, 0xfffff000, v210\n"
    "v_and_b32 v211, 0xfffff000, v211\n"
    "s_waitcnt vmcnt(5)\n"
    "v_perm_b32 v69, v213, v212, s63\n"
    "v_perm_b32 v70, v215, v214, s63\n"
    "v_bitop3_b32 v68, v68, v69, s55 bitop3:0xf6\n"
    "v_bitop3_b32 v68, v68, v70, s55 bitop3:0xf6\n"
    "v_perm_b32 v65, v70, v69, s61\n"
    "v_and_b32 v212, 0xfffff000, v212\n"
    "v_and_b32 v213, 0xfffff000, v213\n"
    "v_and_b32 v214, 0xfffff000, v214\n"
    "v_and_b32 v215, 0xfffff000, v215\n"
    "s_waitcnt vmcnt(4)\n"
    "v_perm_b32 v69, v217, v216, s63\n"
    "v_perm_b32 v70, v219, v218, s63\n"
    "v_bitop3_b32 v68, v68, v69, s55 bitop3:0xf6\n"
    "v_bitop3_b32 v68, v68, v70, s55 bitop3:0xf6\n"
    "v_perm_b32 v66, v70, v69, s61\n"
    "v_and_b32 v216, 0xfffff000, v216\n"
    "v_and_b32 v217, 0xfffff000, v217\n"
    "v_and_b32 v218, 0xfffff000, v218\n"
    "v_and_b32 v219, 0xfffff000, v219\n"
    "v_max3_f32 v71, v64, v65, v66\n"
    "s_waitcnt vmcnt(3)\n"
    "v_perm_b32 v69, v221, v220, s63\n"
    "v_perm_b32 v70, v223, v222, s63\n"
    "v_bitop3_b32 v68, v68, v69, s55 bitop3:0xf6\n"
    "v_bitop3_b32 v68, v68, v70, s55 bitop3:0xf6\n"
    "v_perm_b32 v67, v70, v69, s61\n"
    "v_max_f32 v69, v71, v67\n"
    "v_and_b32 v68, 0x0f0f0000, v68\n"
    "v_cmp_ne_u32 vcc, 0, v68\n"
    "s_cbranch_vccnz .Lretry_oa\n"
    ".Lcont_oa:\n"
    "v_max_f32_dpp v69, v69, v69 row_ror:8 row_mask:0xf bank_mask:0xf bound_ctrl:1\n"
    "v_mov_b32 v70, v69\n"
    "v_and_b32 v220, 0xfffff000, v220\n"
    "v_and_b32 v221, 0xfffff000, v221\n"
    "v_permlane16_swap_b32 v69, v70\n"
    "v_max_f32 v69, v69, v70\n"
    "v_mov_b32 v70, v69\n"
    "v_and_b32 v222, 0xfffff000, v222\n"
    "v_and_b32 v223, 0xfffff000, v223\n"
    "v_permlane32_swap_b32 v69, v70\n"
    "v_max_f32 v69, v69, v70\n"
    "v_sub_f32 v64, v64, v69\n"
    "v_sub_f32 v65, v65, v69\n"
    "v_sub_f32 v66, v66, v69\n"
    "v_sub_f32 v67, v67, v69\n"
    "v_exp_f32 v64, v64\n"
    "v_exp_f32 v65, v65\n"
    "v_exp_f32 v66, v66\n"
    "v_exp_f32 v67, v67\n"
    "v_add_f32 v71, v64, v65\n"
    "v_add_f32 v70, v66, v67\n"
    "v_add_f32 v71, v71, v70\n"
    "v_pk_mul_f32 v[208:209], v[208:209], v[64:65] op_sel_hi:[1,0]\n"
    "v_pk_mul_f32 v[210:211], v[210:211], v[64:65] op_sel_hi:[1,0]\n"
    "v_add_f32_dpp v71, v71, v71 row_ror:8 row_mask:0xf bank_mask:0xf bound_ctrl:1\n"
    "v_mov_b32 v70, v71\n"
    "v_pk_fma_f32 v[208:209], v[212:213], v[64:65], v[208:209] op_sel:[0,1,0]\n"
    "v_pk_fma_f32 v[210:211], v[214:215], v[64:65], v[210:211] op_sel:[0,1,0]\n"
    "v_permlane16_swap_b32 v71, v70\n"
    "v_add_f32 v71, v71, v70\n"
    "v_mov_b32 v70, v71\n"
    "v_pk_fma_f32 v[208:209], v[216:217], v[66:67], v[208:209] op_sel_hi:[1,0,1]\n"
    "v_pk_fma_f32 v[210:211], v[218:219], v[66:67], v[210:211] op_sel_hi:[1,0,1]\n"
    "v_permlane32_swap_b32 v71, v70\n"
    "v_add_f32 v71, v71, v70\n"
    "v_pk_fma_f32 v[208:209], v[220:221], v[66:67], v[208:209] op_sel:[0,1,0]\n"
    "v_pk_fma_f32 v[210:211], v[222:223], v[66:67], v[210:211] op_sel:[0,1,0]\n"
    "v_rcp_f32 v71, v71\n"
    "v_permlane32_swap_b32 v208, v209\n"
    "v_permlane32_swap_b32 v210, v211\n"
    "v_add_f32 v208, v208, v209\n"
    "v_add_f32 v210, v210, v211\n"
    "s_waitcnt lgkmcnt(0)\n"
    "v_perm_b32 v80, v228, v228, v40\n"
    "v_perm_b32 v81, v228, v228, v41\n"
    "v_permlane16_swap_b32 v208, v210\n"
    "v_add_f32 v208, v208, v210\n"
    "v_perm_b32 v82, v228, v228, v42\n"
    "v_perm_b32 v83, v228, v228, v43\n"
    "v_add_f32_dpp v208, v208, v208 row_ror:8 row_mask:0xf bank_mask:0xf bound_ctrl:1\n"
    "v_mul_f32 v208, v208, v71\n"
    "v_cvt_pk_bf16_f32 v208, v208, v208\n"
    "ds_write_b16 v35, v208 offset:9376\n"
    "ds_read_b128 v[176:179], v34 offset:8704\n"
    "ds_read_b128 v[180:183], v34 offset:8800\n"
    "ds_read_b128 v[184:187], v34 offset:8896\n"
    "ds_read_b128 v[188:191], v34 offset:8992\n"
    "ds_read_b128 v[192:195], v34 offset:9088\n"
    "ds_read_b128 v[196:199], v34 offset:9184\n"
    "ds_read_b128 v[200:203], v34 offset:9280\n"
    "ds_read_b128 v[204:207], v34 offset:9376\n"
    "v_mfma_f32_16x16x32_bf16 v[76:79], v[80:83], v[112:115], 0\n"
    "v_perm_b32 v84, v228, v228, v44\n"
    "v_perm_b32 v85, v228, v228, v45\n"
    "v_perm_b32 v86, v228, v228, v46\n"
    "v_perm_b32 v87, v228, v228, v47\n"
    "v_perm_b32 v88, v229, v229, v40\n"
    "v_perm_b32 v89, v229, v229, v41\n"
    "v_mfma_f32_16x16x32_bf16 v[76:79], v[84:87], v[116:119], v[76:79]\n"
    "v_perm_b32 v90, v229, v229, v42\n"
    "v_perm_b32 v91, v229, v229, v43\n"
    "v_perm_b32 v92, v229, v229, v44\n"
    "v_perm_b32 v93, v229, v229, v45\n"
    "v_mfma_f32_16x16x32_bf16 v[76:79], v[88:91], v[120:123], v[76:79]\n"
    "s_waitcnt lgkmcnt(7)\n"
    "v_mfma_f32_16x16x32_bf16 v[76:79], v[176:179], v[144:147], v[76:79]\n"
    "v_perm_b32 v94, v229, v229, v46\n"
    "v_perm_b32 v95, v229, v229, v47\n"
    "s_waitcnt lgkmcnt(6)\n"
    "v_mfma_f32_16x16x32_bf16 v[76:79], v[180:183], v[148:151], v[76:79]\n"
    "v_perm_b32 v96, v230, v230, v40\n"
    "v_perm_b32 v97, v230, v230, v41\n"
    "s_waitcnt lgkmcnt(5)\n"
    "v_mfma_f32_16x16x32_bf16 v[76:79], v[184:187], v[152:155], v[76:79]\n"
    "v_perm_b32 v98, v230, v230, v42\n"
    "v_perm_b32 v99, v230, v230, v43\n"
    "s_waitcnt lgkmcnt(4)\n"
    "v_mfma_f32_16x16x32_bf16 v[76:79], v[188:191], v[156:159], v[76:79]\n"
    "v_perm_b32 v100, v230, v230, v44\n"
    "v_perm_b32 v101, v230, v230, v45\n"
    "s_waitcnt lgkmcnt(3)\n"
    "v_mfma_f32_16x16x32_bf16 v[76:79], v[192:195], v[160:163], v[76:79]\n"
    "v_perm_b32 v102, v230, v230, v46\n"
    "v_perm_b32 v103, v230, v230, v47\n"
    "s_waitcnt lgkmcnt(2)\n"
    "v_mfma_f32_16x16x32_bf16 v[76:79], v[196:199], v[164:167], v[76:79]\n"
    "v_perm_b32 v104, v231, v231, v40\n"
    "v_perm_b32 v105, v231, v231, v41\n"
    "s_waitcnt lgkmcnt(1)\n"
    "v_mfma_f32_16x16x32_bf16 v[76:79], v[200:203], v[168:171], v[76:79]\n"
    "v_perm_b32 v106, v231, v231, v42\n"
    "v_perm_b32 v107, v231, v231, v43\n"
    "s_waitcnt lgkmcnt(0)\n"
    "v_mfma_f32_16x16x32_bf16 v[76:79], v[204:207], v[172:175], v[76:79]\n"
    "v_mfma_f32_16x16x32_bf16 v[76:79], v[92:95], v[124:127], v[76:79]\n"
    "s_waitcnt vmcnt(2)\n"
    "v_mfma_f32_16x16x32_bf16 v[76:79], v[96:99], v[128:131], v[76:79]\n"
    "s_waitcnt vmcnt(1)\n"
    "v_mfma_f32_16x16x32_bf16 v[76:79], v[100:103], v[132:135], v[76:79]\n"
    "s_waitcnt vmcnt(0)\n"
    "v_mfma_f32_16x16x32_bf16 v[76:79], v[104:107], v[136:139], v[76:79]\n"
    "s_mov_b32 exec_hi, 0\n"
    "s_mov_b32 s56, 0x38800000\n"
    "s_mov_b32 s57, 0x44800000\n"
    "s_movk_i32 s58, 0x3fff\n"
    "s_cmp_ge_u32 s49, 2\n"
    "s_nop 2\n"
    "ds_write_b128 v31, v[76:79] offset:4096\n"
    "s_mov_b64 exec, -1\n"
    "s_waitcnt lgkmcnt(0)\n"
    "s_barrier\n"
    "ds_read2st64_b32 v[64:65], v32 offset0:16 offset1:18\n"
    "ds_read2st64_b32 v[66:67], v32 offset0:20 offset1:22\n"
    "s_cbranch_scc1 .Lsidea\n"
    "s_waitcnt lgkmcnt(1)\n"
    "v_add_f32 v64, v64, v65\n"
    "s_waitcnt lgkmcnt(0)\n"
    "v_add_f32 v66, v66, v67\n"
    "v_add_f32 v64, v64, v66\n"
    "v_cmp_le_f32 vcc, s56, |v64|\n"
    "v_cvt_f64_f32 v[70:71], v64\n"
    "v_cmp_gt_f32 s[66:67], s57, |v64|\n"
    "v_cndmask_b32 v65, v234, v235, vcc\n"
    "v_add_f64 v[70:71], v[70:71], s[72:73]\n"
    "v_add_u32 v71, v71, v65\n"
    "v_cndmask_b32 v71, v236, v71, s[66:67]\n"
    "global_atomic_add_x2 v[68:69], v25, v[70:71], s[12:13] sc0 sc1\n"
    "v_cmp_le_f32 vcc, s56, |v27|\n"
    "s_nop 1\n"
    "v_cndmask_b32 v65, 0, 1, vcc\n"
    "s_waitcnt vmcnt(0)\n"
    "v_lshl_add_u64 v[68:69], v[70:71], 0, v[68:69]\n"
    "v_bfe_u32 v66, v69, 14, 6\n"
    "v_lshrrev_b32 v70, 20, v69\n"
    "v_cmp_eq_u32 vcc, 32, v66\n"
    "v_and_or_b32 v69, v69, s58, v237\n"
    "s_and_b64 exec, exec, vcc\n"
    "v_add_f64 v[68:69], v[68:69], v[232:233]\n"
    "s_cbranch_scc0 .Lfb_backa\n"
    "v_cvt_f32_f64 v68, v[68:69]\n"
    "v_cvt_pk_bf16_f32 v68, v68, v68\n"
    "buffer_store_short v68, v26, s[36:39], 0 offen\n"
    "v_or_b32 v65, v65, v70\n"
    "v_add_u32 v65, -1, v65\n"
    "v_cmp_lt_u32 vcc, 62, v65\n"
    "s_cbranch_vccnz .Lfallbacka\n"
    ".Lfb_backa:\n"
    "s_mov_b64 exec, -1\n"
    "v_xor_b32 v66, 25, v66\n"
    "v_or_b32 v26, v26, v66\n"
    "v_cmp_eq_u32 vcc, 0, v26\n"
    "s_cbranch_vccz .Lexita\n"
    "s_mov_b64 exec, vcc\n"
    "buffer_store_dword v37, off, s[12:15], 0 sc1\n"
    ".Lexita:\n"
    "s_endpgm\n"
    ".Lsidea:\n"
    "s_waitcnt lgkmcnt(1)\n"
    "v_add_f32 v64, v64, v65\n"
    "s_waitcnt lgkmcnt(0)\n"
    "v_add_f32 v66, v66, v67\n"
    "v_add_f32 v64, v64, v66\n"
    "v_bfi_b32 v64, 15, v37, v64\n"
    "buffer_store_dword v64, v24, s[12:15], 0 offen sc1\n"
    "s_endpgm\n"
    ".Ltouch:\n"
    "s_branch .Ltouch_back\n"
    ".Lffn:\n"
    "s_and_b32 s56, s51, 15\n"
    "s_lshl_b32 s56, s56, 6\n"
    "v_lshl_add_u32 v7, v2, 4, s56\n"
    "v_lshl_add_u32 v49, v7, 1, s58\n"
    "s_lshl_b32 s57, s49, 8\n"
    "v_add_u32 v8, s57, v5\n"
    "v_cmp_gt_u32 vcc, 896, v8\n"
    "ds_read_b128 v[232:235], v49 offset:16896\n"
    "ds_read_b128 v[236:239], v49 offset:16912\n"
    "s_and_b32 s59, %1, 7\n"
    "s_mul_i32 s56, s59, 896\n"
    "v_mul_u32_u24 v16, 7168, v7\n"
    "v_add3_u32 v16, v16, v8, s56\n"
    "v_cndmask_b32 v16, 2.0, v16, vcc\n"
    "s_cmp_lt_u32 s51, 24\n"
    "s_cselect_b64 vcc, -1, 0\n"
    "v_cndmask_b32 v53, 2.0, v16, vcc\n"
    "s_lshl2_add_u32 s56, s51, s49\n"
    "s_cmp_lg_u32 s56, 127\n"
    "s_cbranch_scc1 .Lnoinit\n"
    "s_lshl_b32 s56, s59, 11\n"
    "s_add_u32 s56, s56, 0x1000\n"
    "v_add_u32 v52, s56, v6\n"
    "v_mov_b32 v212, 0\n"
    "v_mov_b32 v213, 0x2000\n"
    "v_mov_b32 v214, 0\n"
    "v_mov_b32 v215, 0x2000\n"
    "buffer_store_dwordx4 v[212:215], v52, s[12:15], 0 offen sc1\n"
    "buffer_store_dwordx4 v[212:215], v52, s[12:15], 0 offen offset:1024 sc1\n"
    ".Lnoinit:\n"
    "v_perm_b32 v176, v64, v64, v40\n"
    "v_perm_b32 v177, v64, v64, v41\n"
    "v_perm_b32 v178, v64, v64, v42\n"
    "v_perm_b32 v179, v64, v64, v43\n"
    "s_nop 1\n"
    "v_mfma_f32_16x16x32_bf16 v[76:79], v[80:83], v[176:179], 0\n"
    "v_perm_b32 v180, v64, v64, v44\n"
    "v_perm_b32 v181, v64, v64, v45\n"
    "v_perm_b32 v182, v64, v64, v46\n"
    "v_perm_b32 v183, v64, v64, v47\n"
    "s_waitcnt vmcnt(6)\n"
    "s_nop 0\n"
    "v_mfma_f32_16x16x32_bf16 v[76:79], v[84:87], v[180:183], v[76:79]\n"
    "v_perm_b32 v176, v65, v65, v40\n"
    "v_perm_b32 v177, v65, v65, v41\n"
    "v_perm_b32 v178, v65, v65, v42\n"
    "v_perm_b32 v179, v65, v65, v43\n"
    "s_waitcnt vmcnt(5)\n"
    "s_nop 0\n"
    "v_mfma_f32_16x16x32_bf16 v[76:79], v[88:91], v[176:179], v[76:79]\n"
    "v_perm_b32 v180, v65, v65, v44\n"
    "v_perm_b32 v181, v65, v65, v45\n"
    "v_perm_b32 v182, v65, v65, v46\n"
    "v_perm_b32 v183, v65, v65, v47\n"
    "s_waitcnt vmcnt(4)\n"
    "s_nop 0\n"
    "v_mfma_f32_16x16x32_bf16 v[76:79], v[92:95], v[180:183], v[76:79]\n"
    "v_perm_b32 v176, v66, v66, v40\n"
    "v_perm_b32 v177, v66, v66, v41\n"
    "v_perm_b32 v178, v66, v66, v42\n"
    "v_perm_b32 v179, v66, v66, v43\n"
    "s_waitcnt vmcnt(3)\n"
    "s_nop 0\n"
    "v_mfma_f32_16x16x32_bf16 v[76:79], v[96:99], v[176:179], v[76:79]\n"
    "v_perm_b32 v180, v66, v66, v44\n"
    "v_perm_b32 v181, v66, v66, v45\n"
    "v_perm_b32 v182, v66, v66, v46\n"
    "v_perm_b32 v183, v66, v66, v47\n"
    "s_waitcnt vmcnt(2)\n"
    "s_nop 0\n"
    "v_mfma_f32_16x16x32_bf16 v[76:79], v[100:103], v[180:183], v[76:79]\n"
    "v_perm_b32 v176, v67, v67, v40\n"
    "v_perm_b32 v177, v67, v67, v41\n"
    "v_perm_b32 v178, v67, v67, v42\n"
    "v_perm_b32 v179, v67, v67, v43\n"
    "s_waitcnt vmcnt(1)\n"
    "buffer_load_dwordx4 v[112:115], v53, s[20:23], 0 offen nt\n"
    "v_mfma_f32_16x16x32_bf16 v[76:79], v[104:107], v[176:179], v[76:79]\n"
    "v_perm_b32 v180, v67, v67, v44\n"
    "v_perm_b32 v181, v67, v67, v45\n"
    "v_perm_b32 v182, v67, v67, v46\n"
    "v_perm_b32 v183, v67, v67, v47\n"
    "s_waitcnt vmcnt(1)\n"
    "buffer_load_dwordx4 v[116:119], v53, s[20:23], s47 offen offset:3072 nt\n"
    "v_mfma_f32_16x16x32_bf16 v[76:79], v[108:111], v[180:183], v[76:79]\n"
    "s_lshl2_add_u32 s56, s51, s49\n"
    "s_cmp_lg_u32 s56, 127\n"
    "s_cbranch_scc1 .Lnoflag\n"
    "s_lshl_b32 s56, s59, 7\n"
    "v_lshl_add_u32 v28, v1, 2, s56\n"
    "s_waitcnt vmcnt(0)\n"
    "s_mov_b32 exec_hi, 0\n"
    "buffer_store_dword v37, v28, s[12:15], 0 offen offset:1024 sc1\n"
    "s_mov_b64 exec, -1\n"
    ".Lnoflag:\n"
    "s_mov_b32 exec_lo, 0xff00ff\n"
    "s_mov_b32 exec_hi, 0xff00ff00\n"
    "s_cmp_lg_u32 s49, 0\n"
    "s_nop 1\n"
    "ds_write_b128 v29, v[76:79] offset:0\n"
    "s_mov_b64 exec, -1\n"
    "s_waitcnt lgkmcnt(0)\n"
    "s_barrier\n"
    "s_cbranch_scc1 .Lqp_f\n"
    "ds_read_b128 v[64:67], v30 offset:0\n"
    "ds_read_b128 v[68:71], v30 offset:256\n"
    "s_waitcnt lgkmcnt(0)\n"
    "v_pk_add_f32 v[64:65], v[64:65], v[68:69]\n"
    "v_pk_add_f32 v[66:67], v[66:67], v[70:71]\n"
    "s_nop 0\n"
    "v_permlane32_swap_b32 v64, v65\n"
    "v_permlane32_swap_b32 v66, v67\n"
    "v_add_f32 v64, v64, v65\n"
    "v_add_f32 v66, v66, v67\n"
    "s_nop 1\n"
    "v_permlane16_swap_b32 v64, v66\n"
    "v_add_f32 v64, v64, v66\n"
    "v_bfi_b32 v64, 15, v37, v64\n"
    "buffer_store_dword v64, v21, s[12:15], 0 offen sc1\n"
    ".Lqp_f:\n"
    "s_cmp_lt_u32 s51, 24\n"
    "s_cbranch_scc1 .Lfes\n"
    "buffer_load_dwordx4 v[112:115], v16, s[20:23], 0 offen nt\n"
    "buffer_load_dwordx4 v[116:119], v16, s[20:23], s47 offen offset:3072 nt\n"
    ".Lfes:\n"
    "buffer_load_dwordx4 v[120:123], v16, s[20:23], s48 offen offset:2048 nt\n"
    "v_lshlrev_b32 v208, 16, v232\n"
    "v_and_b32 v209, 0xffff0000, v232\n"
    "v_lshlrev_b32 v210, 16, v233\n"
    "v_and_b32 v211, 0xffff0000, v233\n"
    "v_lshlrev_b32 v212, 16, v234\n"
    "v_and_b32 v213, 0xffff0000, v234\n"
    "v_lshlrev_b32 v214, 16, v235\n"
    "v_and_b32 v215, 0xffff0000, v235\n"
    "s_mov_b32 s60, 0x5000\n"
    "buffer_load_dwordx4 v[124:127], v16, s[20:23], s60 offen offset:1024 nt\n"
    "v_pk_add_f32 v[208:209], v[208:209], v[72:73] op_sel_hi:[1,0] neg_lo:[0,1] neg_hi:[0,1]\n"
    "v_pk_add_f32 v[210:211], v[210:211], v[72:73] op_sel_hi:[1,0] neg_lo:[0,1] neg_hi:[0,1]\n"
    "v_pk_add_f32 v[212:213], v[212:213], v[72:73] op_sel_hi:[1,0] neg_lo:[0,1] neg_hi:[0,1]\n"
    "v_pk_add_f32 v[214:215], v[214:215], v[72:73] op_sel_hi:[1,0] neg_lo:[0,1] neg_hi:[0,1]\n"
    "v_cvt_pk_bf16_f32 v232, v208, v209\n"
    "v_cvt_pk_bf16_f32 v233, v210, v211\n"
    "v_cvt_pk_bf16_f32 v234, v212, v213\n"
    "v_cvt_pk_bf16_f32 v235, v214, v215\n"
    "s_mov_b32 s60, 0x7000\n"
    "buffer_load_dwordx4 v[128:131], v16, s[20:23], s60 offen nt\n"
    "v_lshlrev_b32 v208, 16, v232\n"
    "v_and_b32 v209, 0xffff0000, v232\n"
    "v_lshlrev_b32 v210, 16, v233\n"
    "v_and_b32 v211, 0xffff0000, v233\n"
    "v_lshlrev_b32 v212, 16, v234\n"
    "v_and_b32 v213, 0xffff0000, v234\n"
    "v_lshlrev_b32 v214, 16, v235\n"
    "v_and_b32 v215, 0xffff0000, v235\n"
    "buffer_load_dwordx4 v[132:135], v16, s[20:23], s43 offen offset:3072 nt\n"
    "v_pk_mul_f32 v[208:209], v[208:209], v[74:75] op_sel_hi:[1,0]\n"
    "v_pk_mul_f32 v[210:211], v[210:211], v[74:75] op_sel_hi:[1,0]\n"
    "v_pk_mul_f32 v[212:213], v[212:213], v[74:75] op_sel_hi:[1,0]\n"
    "v_pk_mul_f32 v[214:215], v[214:215], v[74:75] op_sel_hi:[1,0]\n"
    "v_cvt_pk_bf16_f32 v232, v208, v209\n"
    "v_cvt_pk_bf16_f32 v233, v210, v211\n"
    "v_cvt_pk_bf16_f32 v234, v212, v213\n"
    "v_cvt_pk_bf16_f32 v235, v214, v215\n"
    "buffer_load_dwordx4 v[136:139], v16, s[20:23], s44 offen offset:2048 nt\n"
    "v_lshlrev_b32 v216, 16, v236\n"
    "v_and_b32 v217, 0xffff0000, v236\n"
    "v_lshlrev_b32 v218, 16, v237\n"
    "v_and_b32 v219, 0xffff0000, v237\n"
    "v_lshlrev_b32 v220, 16, v238\n"
    "v_and_b32 v221, 0xffff0000, v238\n"
    "v_lshlrev_b32 v222, 16, v239\n"
    "v_and_b32 v223, 0xffff0000, v239\n"
    "buffer_load_dwordx4 v[140:143], v16, s[20:23], s45 offen offset:1024 nt\n"
    "v_pk_add_f32 v[216:217], v[216:217], v[72:73] op_sel_hi:[1,0] neg_lo:[0,1] neg_hi:[0,1]\n"
    "v_pk_add_f32 v[218:219], v[218:219], v[72:73] op_sel_hi:[1,0] neg_lo:[0,1] neg_hi:[0,1]\n"
    "v_pk_add_f32 v[220:221], v[220:221], v[72:73] op_sel_hi:[1,0] neg_lo:[0,1] neg_hi:[0,1]\n"
    "v_pk_add_f32 v[222:223], v[222:223], v[72:73] op_sel_hi:[1,0] neg_lo:[0,1] neg_hi:[0,1]\n"
    "v_cvt_pk_bf16_f32 v236, v216, v217\n"
    "v_cvt_pk_bf16_f32 v237, v218, v219\n"
    "v_cvt_pk_bf16_f32 v238, v220, v221\n"
    "v_cvt_pk_bf16_f32 v239, v222, v223\n"
    "v_lshlrev_b32 v216, 16, v236\n"
    "v_and_b32 v217, 0xffff0000, v236\n"
    "v_lshlrev_b32 v218, 16, v237\n"
    "v_and_b32 v219, 0xffff0000, v237\n"
    "v_lshlrev_b32 v220, 16, v238\n"
    "v_and_b32 v221, 0xffff0000, v238\n"
    "v_lshlrev_b32 v222, 16, v239\n"
    "v_and_b32 v223, 0xffff0000, v239\n"
    "v_pk_mul_f32 v[216:217], v[216:217], v[74:75] op_sel_hi:[1,0]\n"
    "v_pk_mul_f32 v[218:219], v[218:219], v[74:75] op_sel_hi:[1,0]\n"
    "v_pk_mul_f32 v[220:221], v[220:221], v[74:75] op_sel_hi:[1,0]\n"
    "v_pk_mul_f32 v[222:223], v[222:223], v[74:75] op_sel_hi:[1,0]\n"
    "v_cvt_pk_bf16_f32 v236, v216, v217\n"
    "v_cvt_pk_bf16_f32 v237, v218, v219\n"
    "v_cvt_pk_bf16_f32 v238, v220, v221\n"
    "v_cvt_pk_bf16_f32 v239, v222, v223\n"
    "s_and_b32 s56, %1, 127\n"
    "s_lshl2_add_u32 s56, s56, s49\n"
    "s_lshl_b32 s56, s56, 9\n"
    "s_add_u32 s56, s56, 0x35000\n"
    "v_lshl_add_u32 v50, v1, 3, s56\n"
    "s_mul_i32 s56, s49, 448\n"
    "v_min_u32 v7, 13, v3\n"
    "v_mul_u32_u24 v8, 112, v2\n"
    "v_lshl_add_u32 v7, v7, 3, v8\n"
    "v_add_u32 v51, s56, v7\n"
    "v_perm_b32 v176, v232, v232, v40\n"
    "v_perm_b32 v177, v232, v232, v41\n"
    "v_perm_b32 v178, v232, v232, v42\n"
    "v_perm_b32 v179, v232, v232, v43\n"
    "s_waitcnt vmcnt(7)\n"
    "buffer_load_dwordx4 v[144:147], v16, s[20:23], s46 offen nt\n"
    "v_mfma_f32_16x16x32_bf16 v[76:79], v[176:179], v[112:115], 0\n"
    "v_perm_b32 v180, v232, v232, v44\n"
    "v_perm_b32 v181, v232, v232, v45\n"
    "v_perm_b32 v182, v232, v232, v46\n"
    "v_perm_b32 v183, v232, v232, v47\n"
    "s_waitcnt vmcnt(7)\n"
    "s_mov_b32 s60, 0xf000\n"
    "buffer_load_dwordx4 v[148:151], v16, s[20:23], s60 offen offset:3072 nt\n"
    "v_mfma_f32_16x16x32_bf16 v[76:79], v[180:183], v[116:119], v[76:79]\n"
    "v_perm_b32 v176, v233, v233, v40\n"
    "v_perm_b32 v177, v233, v233, v41\n"
    "v_perm_b32 v178, v233, v233, v42\n"
    "v_perm_b32 v179, v233, v233, v43\n"
    "s_waitcnt vmcnt(7)\n"
    "s_mov_b32 s60, 0x11000\n"
    "buffer_load_dwordx4 v[152:155], v16, s[20:23], s60 offen offset:2048 nt\n"
    "v_mfma_f32_16x16x32_bf16 v[76:79], v[176:179], v[120:123], v[76:79]\n"
    "v_perm_b32 v180, v233, v233, v44\n"
    "v_perm_b32 v181, v233, v233, v45\n"
    "v_perm_b32 v182, v233, v233, v46\n"
    "v_perm_b32 v183, v233, v233, v47\n"
    "s_waitcnt vmcnt(7)\n"
    "s_mov_b32 s60, 0x13000\n"
    "buffer_load_dwordx4 v[156:159], v16, s[20:23], s60 offen offset:1024 nt\n"
    "v_mfma_f32_16x16x32_bf16 v[76:79], v[180:183], v[124:127], v[76:79]\n"
    "v_perm_b32 v176, v234, v234, v40\n"
    "v_perm_b32 v177, v234, v234, v41\n"
    "v_perm_b32 v178, v234, v234, v42\n"
    "v_perm_b32 v179, v234, v234, v43\n"
    "s_waitcnt vmcnt(7)\n"
    "s_mov_b32 s60, 0x15000\n"
    "buffer_load_dwordx4 v[160:163], v16, s[20:23], s60 offen nt\n"
    "v_mfma_f32_16x16x32_bf16 v[76:79], v[176:179], v[128:131], v[76:79]\n"
    "v_perm_b32 v180, v234, v234, v44\n"
    "v_perm_b32 v181, v234, v234, v45\n"
    "v_perm_b32 v182, v234, v234, v46\n"
    "v_perm_b32 v183, v234, v234, v47\n"
    "s_waitcnt vmcnt(7)\n"
    "s_mov_b32 s60, 0x16000\n"
    "buffer_load_dwordx4 v[164:167], v16, s[20:23], s60 offen offset:3072 nt\n"
    "v_mfma_f32_16x16x32_bf16 v[76:79], v[180:183], v[132:135], v[76:79]\n"
    "v_perm_b32 v176, v235, v235, v40\n"
    "v_perm_b32 v177, v235, v235, v41\n"
    "v_perm_b32 v178, v235, v235, v42\n"
    "v_perm_b32 v179, v235, v235, v43\n"
    "s_waitcnt vmcnt(7)\n"
    "s_mov_b32 s60, 0x18000\n"
    "buffer_load_dwordx4 v[168:171], v16, s[20:23], s60 offen offset:2048 nt\n"
    "v_mfma_f32_16x16x32_bf16 v[76:79], v[176:179], v[136:139], v[76:79]\n"
    "v_perm_b32 v180, v235, v235, v44\n"
    "v_perm_b32 v181, v235, v235, v45\n"
    "v_perm_b32 v182, v235, v235, v46\n"
    "v_perm_b32 v183, v235, v235, v47\n"
    "s_waitcnt vmcnt(7)\n"
    "s_mov_b32 s60, 0x1a000\n"
    "buffer_load_dwordx4 v[172:175], v16, s[20:23], s60 offen offset:1024 nt\n"
    "v_mfma_f32_16x16x32_bf16 v[76:79], v[180:183], v[140:143], v[76:79]\n"
    "v_perm_b32 v176, v236, v236, v40\n"
    "v_perm_b32 v177, v236, v236, v41\n"
    "v_perm_b32 v178, v236, v236, v42\n"
    "v_perm_b32 v179, v236, v236, v43\n"
    "s_waitcnt vmcnt(7)\n"
    "buffer_load_dwordx4 v[80:83], v19, s[28:31], 0 offen nt\n"
    "v_mfma_f32_16x16x32_bf16 v[76:79], v[176:179], v[144:147], v[76:79]\n"
    "v_perm_b32 v180, v236, v236, v44\n"
    "v_perm_b32 v181, v236, v236, v45\n"
    "v_perm_b32 v182, v236, v236, v46\n"
    "v_perm_b32 v183, v236, v236, v47\n"
    "s_waitcnt vmcnt(7)\n"
    "buffer_load_dwordx4 v[84:87], v19, s[28:31], s40 offen nt\n"
    "v_mfma_f32_16x16x32_bf16 v[76:79], v[180:183], v[148:151], v[76:79]\n"
    "v_perm_b32 v176, v237, v237, v40\n"
    "v_perm_b32 v177, v237, v237, v41\n"
    "v_perm_b32 v178, v237, v237, v42\n"
    "v_perm_b32 v179, v237, v237, v43\n"
    "s_waitcnt vmcnt(7)\n"
    "s_nop 0\n"
    "v_mfma_f32_16x16x32_bf16 v[76:79], v[176:179], v[152:155], v[76:79]\n"
    "v_perm_b32 v180, v237, v237, v44\n"
    "v_perm_b32 v181, v237, v237, v45\n"
    "v_perm_b32 v182, v237, v237, v46\n"
    "v_perm_b32 v183, v237, v237, v47\n"
    "s_waitcnt vmcnt(6)\n"
    "s_nop 0\n"
    "v_mfma_f32_16x16x32_bf16 v[76:79], v[180:183], v[156:159], v[76:79]\n"
    "v_perm_b32 v176, v238, v238, v40\n"
    "v_perm_b32 v177, v238, v238, v41\n"
    "v_perm_b32 v178, v238, v238, v42\n"
    "v_perm_b32 v179, v238, v238, v43\n"
    "s_waitcnt vmcnt(5)\n"
    "s_nop 0\n"
    "v_mfma_f32_16x16x32_bf16 v[76:79], v[176:179], v[160:163], v[76:79]\n"
    "v_perm_b32 v180, v238, v238, v44\n"
    "v_perm_b32 v181, v238, v238, v45\n"
    "v_perm_b32 v182, v238, v238, v46\n"
    "v_perm_b32 v183, v238, v238, v47\n"
    "s_waitcnt vmcnt(4)\n"
    "s_nop 0\n"
    "v_mfma_f32_16x16x32_bf16 v[76:79], v[180:183], v[164:167], v[76:79]\n"
    "v_perm_b32 v176, v239, v239, v40\n"
    "v_perm_b32 v177, v239, v239, v41\n"
    "v_perm_b32 v178, v239, v239, v42\n"
    "v_perm_b32 v179, v239, v239, v43\n"
    "s_waitcnt vmcnt(3)\n"
    "buffer_load_dwordx4 v[88:91], v19, s[28:31], s41 offen nt\n"
    "v_mfma_f32_16x16x32_bf16 v[76:79], v[176:179], v[168:171], v[76:79]\n"
    "v_perm_b32 v180, v239, v239, v44\n"
    "v_perm_b32 v181, v239, v239, v45\n"
    "v_perm_b32 v182, v239, v239, v46\n"
    "v_perm_b32 v183, v239, v239, v47\n"
    "s_waitcnt vmcnt(3)\n"
    "buffer_load_dwordx4 v[92:95], v19, s[28:31], s42 offen nt\n"
    "v_mfma_f32_16x16x32_bf16 v[76:79], v[180:183], v[172:175], v[76:79]\n"
    "s_mov_b32 exec_hi, 0\n"
    "s_nop 6\n"
    "ds_write_b128 v31, v[76:79] offset:2048\n"
    "s_mov_b64 exec, -1\n"
    "s_waitcnt lgkmcnt(0)\n"
    "s_barrier\n"
    "ds_read_b64 v[64:65], v51 offset:2048\n"
    "s_waitcnt lgkmcnt(0)\n"
    "v_bfi_b32 v64, 15, v37, v64\n"
    "v_bfi_b32 v65, 15, v37, v65\n"
    "buffer_store_dwordx2 v[64:65], v50, s[12:15], 0 offen sc1\n"
    "s_barrier\n"
    "buffer_load_dwordx4 v[96:99], v19, s[28:31], s43 offen nt\n"
    "buffer_load_dwordx4 v[100:103], v19, s[28:31], s44 offen nt\n"
    "buffer_load_dwordx4 v[104:107], v19, s[28:31], s45 offen nt\n"
    "buffer_load_dwordx4 v[108:111], v19, s[28:31], s46 offen nt\n"
    "s_waitcnt vmcnt(7)\n"
    "v_perm_b32 v144, v84, v80, s61\n"
    "v_perm_b32 v148, v84, v80, s62\n"
    "v_perm_b32 v152, v85, v81, s61\n"
    "v_perm_b32 v156, v85, v81, s62\n"
    "v_perm_b32 v160, v86, v82, s61\n"
    "v_perm_b32 v164, v86, v82, s62\n"
    "v_perm_b32 v168, v87, v83, s61\n"
    "v_perm_b32 v172, v87, v83, s62\n"
    "buffer_load_dwordx4 v[112:115], v20, s[32:35], 0 offen nt\n"
    "buffer_load_dwordx4 v[116:119], v20, s[32:35], s40 offen nt\n"
    "s_waitcnt vmcnt(7)\n"
    "v_perm_b32 v145, v92, v88, s61\n"
    "v_perm_b32 v149, v92, v88, s62\n"
    "v_perm_b32 v153, v93, v89, s61\n"
    "v_perm_b32 v157, v93, v89, s62\n"
    "v_perm_b32 v161, v94, v90, s61\n"
    "v_perm_b32 v165, v94, v90, s62\n"
    "v_perm_b32 v169, v95, v91, s61\n"
    "v_perm_b32 v173, v95, v91, s62\n"
    "buffer_load_dwordx4 v[120:123], v20, s[32:35], s41 offen nt\n"
    "buffer_load_dwordx4 v[124:127], v20, s[32:35], s42 offen nt\n"
    ".Lgatef:\n"
    "s_memrealtime s[56:57]\n"
    "s_waitcnt lgkmcnt(0)\n"
    "s_sub_u32 s56, s56, s70\n"
    "s_cmp_lt_u32 s56, 550\n"
    "s_cbranch_scc1 .Lgatef\n"
    "buffer_load_dwordx4 v[224:227], v23, s[12:15], 0 offen sc1\n"
    "buffer_load_dwordx4 v[228:231], v23, s[12:15], s41 offen sc1\n"
    "s_barrier\n"
    "buffer_load_dwordx4 v[208:211], v22, s[12:15], 0 offen sc1\n"
    "buffer_load_dwordx4 v[212:215], v22, s[12:15], 0 offen offset:512 sc1\n"
    "buffer_load_dwordx4 v[216:219], v22, s[12:15], 0 offen offset:1024 sc1\n"
    "buffer_load_dwordx4 v[220:223], v22, s[12:15], 0 offen offset:1536 sc1\n"
    "s_barrier\n"
    "buffer_load_dwordx4 v[128:131], v20, s[32:35], s43 offen nt\n"
    "buffer_load_dwordx4 v[132:135], v20, s[32:35], s44 offen nt\n"
    "buffer_load_dwordx4 v[136:139], v20, s[32:35], s45 offen nt\n"
    "v_cvt_f64_f32 v[232:233], v27\n"
    "s_mov_b32 s56, 0\n"
    "s_mov_b32 s57, 0xc1502000\n"
    "v_add_f64 v[232:233], v[232:233], s[56:57]\n"
    "v_mov_b32 v234, 0xbea84000\n"
    "v_mov_b32 v235, 0xbeb84000\n"
    "v_mov_b32 v236, 0x4004000\n"
    "v_mov_b32 v237, 0x41500000\n"
    "s_waitcnt vmcnt(15)\n"
    "v_perm_b32 v146, v100, v96, s61\n"
    "v_perm_b32 v150, v100, v96, s62\n"
    "v_perm_b32 v154, v101, v97, s61\n"
    "v_perm_b32 v158, v101, v97, s62\n"
    "v_perm_b32 v162, v102, v98, s61\n"
    "v_perm_b32 v166, v102, v98, s62\n"
    "v_perm_b32 v170, v103, v99, s61\n"
    "v_perm_b32 v174, v103, v99, s62\n"
    "s_waitcnt vmcnt(13)\n"
    "v_perm_b32 v147, v108, v104, s61\n"
    "v_perm_b32 v151, v108, v104, s62\n"
    "v_perm_b32 v155, v109, v105, s61\n"
    "v_perm_b32 v159, v109, v105, s62\n"
    "v_perm_b32 v163, v110, v106, s61\n"
    "v_perm_b32 v167, v110, v106, s62\n"
    "v_perm_b32 v171, v111, v107, s61\n"
    "v_perm_b32 v175, v111, v107, s62\n"
    "s_branch .Lchk_hf\n"
    ".Lretry_hf:\n"
    "s_sub_u32 s54, s54, 1\n"
    "s_cselect_b32 s54, 0, s54\n"
    "s_cbranch_scc1 .Lcont_hf\n"
    "buffer_load_dwordx4 v[224:227], v23, s[12:15], 0 offen sc1\n"
    "buffer_load_dwordx4 v[228:231], v23, s[12:15], s41 offen sc1\n"
    "s_waitcnt vmcnt(0)\n"
    ".Lchk_hf:\n"
    "s_waitcnt vmcnt(8)\n"
    "v_xor_b32 v224, s52, v224\n"
    "v_xor_b32 v225, s52, v225\n"
    "v_xor_b32 v226, s52, v226\n"
    "v_or3_b32 v68, v224, v225, v226\n"
    "v_xor_b32 v227, s52, v227\n"
    "s_waitcnt vmcnt(7)\n"
    "v_xor_b32 v228, s52, v228\n"
    "v_or3_b32 v68, v68, v227, v228\n"
    "v_xor_b32 v229, s52, v229\n"
    "v_xor_b32 v230, s52, v230\n"
    "v_or3_b32 v68, v68, v229, v230\n"
    "v_xor_b32 v231, s52, v231\n"
    "v_bitop3_b32 v68, v68, v231, 15 bitop3:0xa8\n"
    "v_cmp_ne_u32 vcc, 0, v68\n"
    "s_cbranch_vccnz .Lretry_hf\n"
    ".Lcont_hf:\n"
    "v_pk_add_f32 v[224:225], v[224:225], v[228:229]\n"
    "v_pk_add_f32 v[226:227], v[226:227], v[230:231]\n"
    "s_nop 0\n"
    "v_permlane32_swap_b32 v224, v225\n"
    "v_permlane32_swap_b32 v226, v227\n"
    "v_add_f32 v224, v224, v225\n"
    "v_add_f32 v226, v226, v227\n"
    "s_nop 1\n"
    "v_permlane16_swap_b32 v224, v226\n"
    "v_add_f32 v224, v224, v226\n"
    "s_nop 1\n"
    "v_add_f32_dpp v224, v224, v224 row_ror:8 row_mask:0xf bank_mask:0xf bound_ctrl:1\n"
    "v_cvt_pk_bf16_f32 v224, v224, v224\n"
    "v_lshlrev_b32 v224, 16, v224\n"
    "v_mul_f32 v225, 0xbfb8aa3b, v224\n"
    "v_exp_f32 v225, v225\n"
    "s_nop 0\n"
    "v_add_f32 v225, 1.0, v225\n"
    "v_rcp_f32 v225, v225\n"
    "s_nop 0\n"
    "v_mul_f32 v224, v224, v225\n"
    "v_cvt_pk_bf16_f32 v224, v224, v224\n"
    "ds_write_b16 v33, v224 offset:10240\n"
    "ds_read_b128 v[228:231], v36 offset:10240\n"
    "s_branch .Lchk_of\n"
    ".Lretry_of:\n"
    "s_sub_u32 s54, s54, 1\n"
    "s_cselect_b32 s54, 0, s54\n"
    "s_cbranch_scc1 .Lcont_of\n"
    "buffer_load_dwordx4 v[208:211], v22, s[12:15], 0 offen sc1\n"
    "buffer_load_dwordx4 v[212:215], v22, s[12:15], 0 offen offset:512 sc1\n"
    "buffer_load_dwordx4 v[216:219], v22, s[12:15], 0 offen offset:1024 sc1\n"
    "buffer_load_dwordx4 v[220:223], v22, s[12:15], 0 offen offset:1536 sc1\n"
    "s_waitcnt vmcnt(0)\n"
    ".Lchk_of:\n"
    "s_waitcnt vmcnt(6)\n"
    "v_perm_b32 v69, v209, v208, s63\n"
    "v_perm_b32 v70, v211, v210, s63\n"
    "v_xor_b32 v68, s55, v69\n"
    "v_bitop3_b32 v68, v68, v70, s55 bitop3:0xf6\n"
    "v_perm_b32 v64, v70, v69, s61\n"
    "v_and_b32 v208, 0xfffff000, v208\n"
    "v_and_b32 v209, 0xfffff000, v209\n"
    "v_and_b32 v210, 0xfffff000, v210\n"
    "v_and_b32 v211, 0xfffff000, v211\n"
    "s_waitcnt vmcnt(5)\n"
    "v_perm_b32 v69, v213, v212, s63\n"
    "v_perm_b32 v70, v215, v214, s63\n"
    "v_bitop3_b32 v68, v68, v69, s55 bitop3:0xf6\n"
    "v_bitop3_b32 v68, v68, v70, s55 bitop3:0xf6\n"
    "v_perm_b32 v65, v70, v69, s61\n"
    "v_and_b32 v212, 0xfffff000, v212\n"
    "v_and_b32 v213, 0xfffff000, v213\n"
    "v_and_b32 v214, 0xfffff000, v214\n"
    "v_and_b32 v215, 0xfffff000, v215\n"
    "s_waitcnt vmcnt(4)\n"
    "v_perm_b32 v69, v217, v216, s63\n"
    "v_perm_b32 v70, v219, v218, s63\n"
    "v_bitop3_b32 v68, v68, v69, s55 bitop3:0xf6\n"
    "v_bitop3_b32 v68, v68, v70, s55 bitop3:0xf6\n"
    "v_perm_b32 v66, v70, v69, s61\n"
    "v_and_b32 v216, 0xfffff000, v216\n"
    "v_and_b32 v217, 0xfffff000, v217\n"
    "v_and_b32 v218, 0xfffff000, v218\n"
    "v_and_b32 v219, 0xfffff000, v219\n"
    "v_max3_f32 v71, v64, v65, v66\n"
    "s_waitcnt vmcnt(3)\n"
    "v_perm_b32 v69, v221, v220, s63\n"
    "v_perm_b32 v70, v223, v222, s63\n"
    "v_bitop3_b32 v68, v68, v69, s55 bitop3:0xf6\n"
    "v_bitop3_b32 v68, v68, v70, s55 bitop3:0xf6\n"
    "v_perm_b32 v67, v70, v69, s61\n"
    "v_max_f32 v69, v71, v67\n"
    "v_and_b32 v68, 0x0f0f0000, v68\n"
    "v_cmp_ne_u32 vcc, 0, v68\n"
    "s_cbranch_vccnz .Lretry_of\n"
    ".Lcont_of:\n"
    "v_max_f32_dpp v69, v69, v69 row_ror:8 row_mask:0xf bank_mask:0xf bound_ctrl:1\n"
    "v_mov_b32 v70, v69\n"
    "v_and_b32 v220, 0xfffff000, v220\n"
    "v_and_b32 v221, 0xfffff000, v221\n"
    "v_permlane16_swap_b32 v69, v70\n"
    "v_max_f32 v69, v69, v70\n"
    "v_mov_b32 v70, v69\n"
    "v_and_b32 v222, 0xfffff000, v222\n"
    "v_and_b32 v223, 0xfffff000, v223\n"
    "v_permlane32_swap_b32 v69, v70\n"
    "v_max_f32 v69, v69, v70\n"
    "v_sub_f32 v64, v64, v69\n"
    "v_sub_f32 v65, v65, v69\n"
    "v_sub_f32 v66, v66, v69\n"
    "v_sub_f32 v67, v67, v69\n"
    "v_exp_f32 v64, v64\n"
    "v_exp_f32 v65, v65\n"
    "v_exp_f32 v66, v66\n"
    "v_exp_f32 v67, v67\n"
    "v_add_f32 v71, v64, v65\n"
    "v_add_f32 v70, v66, v67\n"
    "v_add_f32 v71, v71, v70\n"
    "v_pk_mul_f32 v[208:209], v[208:209], v[64:65] op_sel_hi:[1,0]\n"
    "v_pk_mul_f32 v[210:211], v[210:211], v[64:65] op_sel_hi:[1,0]\n"
    "v_add_f32_dpp v71, v71, v71 row_ror:8 row_mask:0xf bank_mask:0xf bound_ctrl:1\n"
    "v_mov_b32 v70, v71\n"
    "v_pk_fma_f32 v[208:209], v[212:213], v[64:65], v[208:209] op_sel:[0,1,0]\n"
    "v_pk_fma_f32 v[210:211], v[214:215], v[64:65], v[210:211] op_sel:[0,1,0]\n"
    "v_permlane16_swap_b32 v71, v70\n"
    "v_add_f32 v71, v71, v70\n"
    "v_mov_b32 v70, v71\n"
    "v_pk_fma_f32 v[208:209], v[216:217], v[66:67], v[208:209] op_sel_hi:[1,0,1]\n"
    "v_pk_fma_f32 v[210:211], v[218:219], v[66:67], v[210:211] op_sel_hi:[1,0,1]\n"
    "v_permlane32_swap_b32 v71, v70\n"
    "v_add_f32 v71, v71, v70\n"
    "v_pk_fma_f32 v[208:209], v[220:221], v[66:67], v[208:209] op_sel:[0,1,0]\n"
    "v_pk_fma_f32 v[210:211], v[222:223], v[66:67], v[210:211] op_sel:[0,1,0]\n"
    "v_rcp_f32 v71, v71\n"
    "v_permlane32_swap_b32 v208, v209\n"
    "v_permlane32_swap_b32 v210, v211\n"
    "v_add_f32 v208, v208, v209\n"
    "v_add_f32 v210, v210, v211\n"
    "s_waitcnt lgkmcnt(0)\n"
    "v_perm_b32 v80, v228, v228, v40\n"
    "v_perm_b32 v81, v228, v228, v41\n"
    "v_permlane16_swap_b32 v208, v210\n"
    "v_add_f32 v208, v208, v210\n"
    "v_perm_b32 v82, v228, v228, v42\n"
    "v_perm_b32 v83, v228, v228, v43\n"
    "v_add_f32_dpp v208, v208, v208 row_ror:8 row_mask:0xf bank_mask:0xf bound_ctrl:1\n"
    "v_mul_f32 v208, v208, v71\n"
    "v_cvt_pk_bf16_f32 v208, v208, v208\n"
    "ds_write_b16 v35, v208 offset:9376\n"
    "ds_read_b128 v[176:179], v34 offset:8704\n"
    "ds_read_b128 v[180:183], v34 offset:8800\n"
    "ds_read_b128 v[184:187], v34 offset:8896\n"
    "ds_read_b128 v[188:191], v34 offset:8992\n"
    "ds_read_b128 v[192:195], v34 offset:9088\n"
    "ds_read_b128 v[196:199], v34 offset:9184\n"
    "ds_read_b128 v[200:203], v34 offset:9280\n"
    "ds_read_b128 v[204:207], v34 offset:9376\n"
    "v_mfma_f32_16x16x32_bf16 v[76:79], v[80:83], v[112:115], 0\n"
    "v_perm_b32 v84, v228, v228, v44\n"
    "v_perm_b32 v85, v228, v228, v45\n"
    "v_perm_b32 v86, v228, v228, v46\n"
    "v_perm_b32 v87, v228, v228, v47\n"
    "v_perm_b32 v88, v229, v229, v40\n"
    "v_perm_b32 v89, v229, v229, v41\n"
    "v_mfma_f32_16x16x32_bf16 v[76:79], v[84:87], v[116:119], v[76:79]\n"
    "v_perm_b32 v90, v229, v229, v42\n"
    "v_perm_b32 v91, v229, v229, v43\n"
    "v_perm_b32 v92, v229, v229, v44\n"
    "v_perm_b32 v93, v229, v229, v45\n"
    "v_mfma_f32_16x16x32_bf16 v[76:79], v[88:91], v[120:123], v[76:79]\n"
    "s_waitcnt lgkmcnt(7)\n"
    "v_mfma_f32_16x16x32_bf16 v[76:79], v[176:179], v[144:147], v[76:79]\n"
    "v_perm_b32 v94, v229, v229, v46\n"
    "v_perm_b32 v95, v229, v229, v47\n"
    "s_waitcnt lgkmcnt(6)\n"
    "v_mfma_f32_16x16x32_bf16 v[76:79], v[180:183], v[148:151], v[76:79]\n"
    "v_perm_b32 v96, v230, v230, v40\n"
    "v_perm_b32 v97, v230, v230, v41\n"
    "s_waitcnt lgkmcnt(5)\n"
    "v_mfma_f32_16x16x32_bf16 v[76:79], v[184:187], v[152:155], v[76:79]\n"
    "v_perm_b32 v98, v230, v230, v42\n"
    "v_perm_b32 v99, v230, v230, v43\n"
    "s_waitcnt lgkmcnt(4)\n"
    "v_mfma_f32_16x16x32_bf16 v[76:79], v[188:191], v[156:159], v[76:79]\n"
    "v_perm_b32 v100, v230, v230, v44\n"
    "v_perm_b32 v101, v230, v230, v45\n"
    "s_waitcnt lgkmcnt(3)\n"
    "v_mfma_f32_16x16x32_bf16 v[76:79], v[192:195], v[160:163], v[76:79]\n"
    "v_perm_b32 v102, v230, v230, v46\n"
    "v_perm_b32 v103, v230, v230, v47\n"
    "s_waitcnt lgkmcnt(2)\n"
    "v_mfma_f32_16x16x32_bf16 v[76:79], v[196:199], v[164:167], v[76:79]\n"
    "v_perm_b32 v104, v231, v231, v40\n"
    "v_perm_b32 v105, v231, v231, v41\n"
    "s_waitcnt lgkmcnt(1)\n"
    "v_mfma_f32_16x16x32_bf16 v[76:79], v[200:203], v[168:171], v[76:79]\n"
    "v_perm_b32 v106, v231, v231, v42\n"
    "v_perm_b32 v107, v231, v231, v43\n"
    "s_waitcnt lgkmcnt(0)\n"
    "v_mfma_f32_16x16x32_bf16 v[76:79], v[204:207], v[172:175], v[76:79]\n"
    "v_mfma_f32_16x16x32_bf16 v[76:79], v[92:95], v[124:127], v[76:79]\n"
    "s_waitcnt vmcnt(2)\n"
    "v_mfma_f32_16x16x32_bf16 v[76:79], v[96:99], v[128:131], v[76:79]\n"
    "s_waitcnt vmcnt(1)\n"
    "v_mfma_f32_16x16x32_bf16 v[76:79], v[100:103], v[132:135], v[76:79]\n"
    "s_waitcnt vmcnt(0)\n"
    "v_mfma_f32_16x16x32_bf16 v[76:79], v[104:107], v[136:139], v[76:79]\n"
    "s_mov_b32 exec_hi, 0\n"
    "s_mov_b32 s56, 0x38800000\n"
    "s_mov_b32 s57, 0x44800000\n"
    "s_movk_i32 s58, 0x3fff\n"
    "s_cmp_ge_u32 s49, 2\n"
    "s_nop 2\n"
    "ds_write_b128 v31, v[76:79] offset:4096\n"
    "s_mov_b64 exec, -1\n"
    "s_waitcnt lgkmcnt(0)\n"
    "s_barrier\n"
    "ds_read2st64_b32 v[64:65], v32 offset0:16 offset1:18\n"
    "ds_read2st64_b32 v[66:67], v32 offset0:20 offset1:22\n"
    "s_cbranch_scc1 .Lsidef\n"
    "s_waitcnt lgkmcnt(1)\n"
    "v_add_f32 v64, v64, v65\n"
    "s_waitcnt lgkmcnt(0)\n"
    "v_add_f32 v66, v66, v67\n"
    "v_add_f32 v64, v64, v66\n"
    "v_cmp_le_f32 vcc, s56, |v64|\n"
    "v_cvt_f64_f32 v[70:71], v64\n"
    "v_cmp_gt_f32 s[66:67], s57, |v64|\n"
    "v_cndmask_b32 v65, v234, v235, vcc\n"
    "v_add_f64 v[70:71], v[70:71], s[72:73]\n"
    "v_add_u32 v71, v71, v65\n"
    "v_cndmask_b32 v71, v236, v71, s[66:67]\n"
    "global_atomic_add_x2 v[68:69], v25, v[70:71], s[12:13] sc0 sc1\n"
    "v_cmp_le_f32 vcc, s56, |v27|\n"
    "s_nop 1\n"
    "v_cndmask_b32 v65, 0, 1, vcc\n"
    "s_waitcnt vmcnt(0)\n"
    "v_lshl_add_u64 v[68:69], v[70:71], 0, v[68:69]\n"
    "v_bfe_u32 v66, v69, 14, 6\n"
    "v_lshrrev_b32 v70, 20, v69\n"
    "v_cmp_eq_u32 vcc, 32, v66\n"
    "v_and_or_b32 v69, v69, s58, v237\n"
    "s_and_b64 exec, exec, vcc\n"
    "v_add_f64 v[68:69], v[68:69], v[232:233]\n"
    "s_cbranch_scc0 .Lfb_backf\n"
    "v_cvt_f32_f64 v68, v[68:69]\n"
    "v_cvt_pk_bf16_f32 v68, v68, v68\n"
    "buffer_store_short v68, v26, s[36:39], 0 offen\n"
    "v_or_b32 v65, v65, v70\n"
    "v_add_u32 v65, -1, v65\n"
    "v_cmp_lt_u32 vcc, 62, v65\n"
    "s_cbranch_vccnz .Lfallbackf\n"
    ".Lfb_backf:\n"
    "s_mov_b64 exec, -1\n"
    "v_xor_b32 v66, 25, v66\n"
    "v_or_b32 v26, v26, v66\n"
    "v_cmp_eq_u32 vcc, 0, v26\n"
    "s_cbranch_vccz .Lexitf\n"
    "s_mov_b64 exec, vcc\n"
    "buffer_store_dword v37, off, s[12:15], 0 sc1\n"
    ".Lexitf:\n"
    "s_endpgm\n"
    ".Lsidef:\n"
    "s_waitcnt lgkmcnt(1)\n"
    "v_add_f32 v64, v64, v65\n"
    "s_waitcnt lgkmcnt(0)\n"
    "v_add_f32 v66, v66, v67\n"
    "v_add_f32 v64, v64, v66\n"
    "v_bfi_b32 v64, 15, v37, v64\n"
    "buffer_store_dword v64, v24, s[12:15], 0 offen sc1\n"
    "s_endpgm\n"
    ".Lfallbacka:\n"
    "s_mov_b64 s[72:73], exec\n"
    "s_mov_b64 s[66:67], vcc\n"
    "s_mov_b64 exec, -1\n"
    "v_mov_b32 v70, 0\n"
    "v_subrev_u32 v24, 0x15000, v24\n"
    "v_and_b32 v24, 0xffffc1ff, v24\n"
    "s_mov_b32 s56, 0x15000\n"
    ".Lfb_loopa:\n"
    "buffer_load_dword v64, v24, s[12:15], s56 offen sc1\n"
    "s_waitcnt vmcnt(0)\n"
    "v_xor_b32 v64, s52, v64\n"
    "v_and_b32 v65, 15, v64\n"
    "v_cmp_ne_u32 vcc, 0, v65\n"
    "s_cbranch_vccz .Lfb_oka\n"
    "s_sub_u32 s54, s54, 1\n"
    "s_cbranch_scc0 .Lfb_loopa\n"
    "s_mov_b32 s54, 0\n"
    ".Lfb_oka:\n"
    "v_add_f32 v70, v70, v64\n"
    "s_add_u32 s56, s56, 512\n"
    "s_cmp_lt_u32 s56, 0x19000\n"
    "s_cbranch_scc1 .Lfb_loopa\n"
    "s_mov_b64 exec, s[66:67]\n"
    "v_add_f32 v70, v70, v27\n"
    "v_cvt_pk_bf16_f32 v70, v70, v70\n"
    "buffer_store_short v70, v26, s[36:39], 0 offen\n"
    "s_mov_b64 exec, s[72:73]\n"
    "s_branch .Lfb_backa\n"
    ".Lfallbackf:\n"
    "s_mov_b64 s[72:73], exec\n"
    "s_mov_b64 s[66:67], vcc\n"
    "s_mov_b64 exec, -1\n"
    "v_mov_b32 v70, 0\n"
    "v_subrev_u32 v24, 0x15000, v24\n"
    "v_and_b32 v24, 0xffffc1ff, v24\n"
    "s_mov_b32 s56, 0x15000\n"
    ".Lfb_loopf:\n"
    "buffer_load_dword v64, v24, s[12:15], s56 offen sc1\n"
    "s_waitcnt vmcnt(0)\n"
    "v_xor_b32 v64, s52, v64\n"
    "v_and_b32 v65, 15, v64\n"
    "v_cmp_ne_u32 vcc, 0, v65\n"
    "s_cbranch_vccz .Lfb_okf\n"
    "s_sub_u32 s54, s54, 1\n"
    "s_cbranch_scc0 .Lfb_loopf\n"
    "s_mov_b32 s54, 0\n"
    ".Lfb_okf:\n"
    "v_add_f32 v70, v70, v64\n"
    "s_add_u32 s56, s56, 512\n"
    "s_cmp_lt_u32 s56, 0x19000\n"
    "s_cbranch_scc1 .Lfb_loopf\n"
    "s_mov_b64 exec, s[66:67]\n"
    "v_add_f32 v70, v70, v27\n"
    "v_cvt_pk_bf16_f32 v70, v70, v70\n"
    "buffer_store_short v70, v26, s[36:39], 0 offen\n"
    "s_mov_b64 exec, s[72:73]\n"
    "s_branch .Lfb_backf\n"
    ".Lend:\n"
    :
    : "s"(__builtin_amdgcn_kernarg_segment_ptr()), "s"(blockIdx.x), "v"(threadIdx.x),
      "s"((__attribute__((address_space(3))) unsigned char*)smem)
    : "v1", "v2", "v3", "v4", "v5", "v6", "v7", "v8", "v9", "v10", "v11", "v12", "v13", "v14", "v15", "v16", "v17", "v18", "v19", "v20",
      "v21", "v22", "v23", "v24", "v25", "v26", "v27", "v28", "v29", "v30", "v31", "v32", "v33", "v34", "v35", "v36", "v37", "v38", "v39", "v40",
      "v41", "v42", "v43", "v44", "v45", "v46", "v47", "v48", "v49", "v50", "v51", "v52", "v53", "v54", "v55", "v56", "v57", "v58", "v59", "v60",
      "v61", "v62", "v63", "v64", "v65", "v66", "v67", "v68", "v69", "v70", "v71", "v72", "v73", "v74", "v75", "v76", "v77", "v78", "v79", "v80",
      "v81", "v82", "v83", "v84", "v85", "v86", "v87", "v88", "v89", "v90", "v91", "v92", "v93", "v94", "v95", "v96", "v97", "v98", "v99", "v100",
      "v101", "v102", "v103", "v104", "v105", "v106", "v107", "v108", "v109", "v110", "v111", "v112", "v113", "v114", "v115", "v116", "v117", "v118", "v119", "v120",
      "v121", "v122", "v123", "v124", "v125", "v126", "v127", "v128", "v129", "v130", "v131", "v132", "v133", "v134", "v135", "v136", "v137", "v138", "v139", "v140",
      "v141", "v142", "v143", "v144", "v145", "v146", "v147", "v148", "v149", "v150", "v151", "v152", "v153", "v154", "v155", "v156", "v157", "v158", "v159", "v160",
      "v161", "v162", "v163", "v164", "v165", "v166", "v167", "v168", "v169", "v170", "v171", "v172", "v173", "v174", "v175", "v176", "v177", "v178", "v179", "v180",
      "v181", "v182", "v183", "v184", "v185", "v186", "v187", "v188", "v189", "v190", "v191", "v192", "v193", "v194", "v195", "v196", "v197", "v198", "v199", "v200",
      "v201", "v202", "v203", "v204", "v205", "v206", "v207", "v208", "v209", "v210", "v211", "v212", "v213", "v214", "v215", "v216", "v217", "v218", "v219", "v220",
      "v221", "v222", "v223", "v224", "v225", "v226", "v227", "v228", "v229", "v230", "v231", "v232", "v233", "v234", "v235", "v236", "v237", "v238", "v239", "s4",
      "s5", "s6", "s7", "s8", "s9", "s10", "s11", "s12", "s13", "s14", "s15", "s16", "s17", "s18", "s19", "s20", "s21", "s22", "s23", "s24",
      "s25", "s26", "s27", "s28", "s29", "s30", "s31", "s33", "s34", "s35", "s36", "s37", "s38", "s39", "s40", "s41", "s42", "s43", "s44", "s45",
      "s46", "s47", "s48", "s49", "s50", "s51", "s52", "s53", "s54", "s55", "s56", "s57", "s58", "s59", "s60", "s61", "s62", "s63", "s64", "s65",
      "s66", "s67", "s68", "s69", "s70", "s71", "s72", "s73", "vcc", "scc", "memory");
}

extern "C" void kernel_launch(void* const* d_in, const int* in_sizes, int n_in, void* d_out, int out_size, void* d_ws, size_t ws_size,
                              hipStream_t stream) {
  const uint32_t F = 0x00020000u;
  Args a;
  a.x = {d_in[0], 2048u, F};
  a.k = {d_in[1], 67108864u, F};
  a.v = {d_in[2], 67108864u, F};
  a.wq = {d_in[3], 8388608u, F};
  a.wo = {d_in[4], 8388608u, F};
  a.w1 = {d_in[6], 7340032u, F};
  a.w2 = {d_in[7], 7340032u, F};
  a.out = {d_out, 2048u, F};
  a.ws = {d_ws, 1003520u, F};
  hipLaunchKernelGGL(fused_decode, dim3(256), dim3(256), 0, stream, a);
}
```
